```python
import torch

def _softmax(x, axis=-1):
    return torch.softmax(x, dim=axis)
def _gelu(x):
    return torch.nn.functional.gelu(x, approximate="tanh")
def _silu(x):
    return torch.nn.functional.silu(x)
def _sigmoid(x):
    return torch.sigmoid(x)
def _layernorm(x, axis=-1, eps=1e-5):
    m = x.mean(dim=axis, keepdim=True)
    v = x.var(dim=axis, keepdim=True, unbiased=False)
    return (x - m) / torch.sqrt(v + eps)
def _scatter_add(tgt, idx, src, axis=0):
    return tgt.clone().index_add_(axis, idx, src)
def _segment_sum(src, seg, n):
    out = torch.zeros((n,) + src.shape[1:], dtype=src.dtype)
    return out.index_add_(0, seg, src)
def _pad(x, pw):
    flat = tuple(v for pair in reversed(pw) for v in pair)
    return torch.nn.functional.pad(x, flat)

kernel_name = 'standalone__cross_network_dcn_v2__recsys'

def setup_inputs():

    _w = (lambda a: (lambda t: t.to(torch.bfloat16) if t.is_floating_point() else t)(torch.as_tensor(a)))
    B, F, D, L = 16384, 100, 32, 6
    Din = min(F * D, 512)
    R = max(Din // 4, 2)
    sc = Din ** -0.5
    return {
        "x0": _w((lambda *s: torch.randn(*s, dtype=torch.bfloat16) * 0.05)(B, Din)),
        "U":  _w((lambda *s: torch.randn(*s, dtype=torch.bfloat16) * 1.6)(L, Din, R) * sc),
        "V":  _w((lambda *s: torch.randn(*s, dtype=torch.bfloat16) * 1.6)(L, R, Din) * sc),
        "bs": _w((lambda *s: torch.randn(*s, dtype=torch.bfloat16) * 0.05)(L, Din)),
    }

def reference(x0, U, V, bs):
    x = x0
    for l in range(U.shape[0]):
        h = torch.einsum('rd,bd->br', V[l], x)
        h = torch.einsum('dr,br->bd', U[l], h)
        x = x0 * (h + bs[l]) + x
    return x
```

```cpp
#include <hip/hip_runtime.h>
#include <hip/hip_bf16.h>

namespace {

typedef unsigned v4u __attribute__((ext_vector_type(4)));
typedef float f32x4 __attribute__((ext_vector_type(4)));
typedef float f32x2 __attribute__((ext_vector_type(2)));
typedef short bf16x8 __attribute__((ext_vector_type(8)));
typedef __bf16 bf16x2 __attribute__((ext_vector_type(2)));
#define LAS __attribute__((address_space(3)))
#define DI __device__ __forceinline__
#define SBAR() __builtin_amdgcn_sched_barrier(0)

constexpr int kD = 512, kR = 128, kL = 6;
constexpr int kM = 64;
constexpr int XIMG = 0;
constexpr int HIMG = 65536;
constexpr int RING = 81920;
constexpr int BIAS = 147456;
constexpr int SCR = 159744;
constexpr int LDS_BYTES = 160768;
constexpr int W_LAYER = kD * kR * 2;

DI f32x4 mfma16(v4u a, v4u b, f32x4 c) {
  return __builtin_amdgcn_mfma_f32_16x16x32_bf16(__builtin_bit_cast(bf16x8, a), __builtin_bit_cast(bf16x8, b), c, 0, 0, 0);
}
DI unsigned pk2(float a, float b) {
  f32x2 v = {a, b};
  return __builtin_bit_cast(unsigned, __builtin_convertvector(v, bf16x2));
}
DI float bflo(unsigned u) { return __builtin_bit_cast(float, u << 16); }
DI float bfhi(unsigned u) { return __builtin_bit_cast(float, u & 0xffff0000u); }
template <int N> DI void wait_vm() { asm volatile("s_waitcnt vmcnt(%0)" ::"i"(N) : "memory"); }
DI void wait_lgkm0() { asm volatile("s_waitcnt lgkmcnt(0)" ::: "memory"); }
DI void barrier() {
  asm volatile("s_waitcnt lgkmcnt(0)" ::: "memory");
  __builtin_amdgcn_s_barrier();
  asm volatile("" ::: "memory");
}

__global__ void __launch_bounds__(256, 1)
dcn_kernel(const unsigned char* __restrict__ x0g, const unsigned char* __restrict__ Ug, const unsigned char* __restrict__ Vg,
           const unsigned short* __restrict__ bsg, unsigned char* __restrict__ outg) {
  extern __shared__ __attribute__((aligned(16))) unsigned char smem_[];
  LAS unsigned char* smem = (LAS unsigned char*)smem_;
  const int tid = threadIdx.x, lane = tid & 63;
  const int w = __builtin_amdgcn_readfirstlane(tid >> 6);
  const int blk = blockIdx.x;
  const int c16 = lane & 15, g = lane >> 4;

  const __amdgpu_buffer_rsrc_t rsX = __builtin_amdgcn_make_buffer_rsrc((void*)(x0g + (size_t)blk * (kM * kD * 2)), 0, kM * kD * 2, 0x00020000);
  const __amdgpu_buffer_rsrc_t rsU = __builtin_amdgcn_make_buffer_rsrc((void*)Ug, 0, kL * W_LAYER, 0x00020000);
  const __amdgpu_buffer_rsrc_t rsV = __builtin_amdgcn_make_buffer_rsrc((void*)Vg, 0, kL * W_LAYER, 0x00020000);

#pragma unroll
  for (int p = 0; p < 16; ++p) {
    const int row = 4 * p + g;
    const int vo = row * 1024 + ((c16 ^ (row & 15)) << 4);
    __builtin_amdgcn_raw_ptr_buffer_load_lds(rsX, (LAS void*)(smem + XIMG + w * 16384 + p * 1024), 16, vo, w * 256, 0, 0);
  }
  int voV[4], voU[4];
#pragma unroll
  for (int p = 0; p < 4; ++p) {
    const int ir = 8 * p + (lane >> 3);
    const int T = ir >> 4, i = ir & 15;
    const int rho = 8 * (i >> 2) + 4 * T + (i & 3);
    const int ch = (lane & 7) ^ ((i >> 1) & 7);
    voV[p] = rho * 1024 + ch * 16;
    voU[p] = rho * 256 + ch * 16;
  }
  const int wbase = w * 32768;
  auto issue_unit = [&](int l, int t) {
    const int slot = t & 3;
    LAS unsigned char* dst = smem + RING + w * 16384 + slot * 4096;
    if (t < 8) {
      const int so = l * W_LAYER + wbase + t * 128;
#pragma unroll
      for (int p = 0; p < 4; ++p) __builtin_amdgcn_raw_ptr_buffer_load_lds(rsV, (LAS void*)(dst + p * 1024), 16, voV[p], so, 0, 0);
    } else {
      const int so = l * W_LAYER + wbase + ((t - 8) >> 1) * 8192 + ((t - 8) & 1) * 128;
#pragma unroll
      for (int p = 0; p < 4; ++p) __builtin_amdgcn_raw_ptr_buffer_load_lds(rsU, (LAS void*)(dst + p * 1024), 16, voU[p], so, 0, 0);
    }
  };
  issue_unit(0, 0);
  issue_unit(0, 1);
  issue_unit(0, 2);
  {
    const int part = (blk >> 3) & 31;
    const int vo = part * 4096 + (lane & 31) * 128;
#pragma unroll
    for (int j = 0; j < 2; ++j) {
      const int l = w + 4 * j;
      if (lane < 32) __builtin_amdgcn_raw_ptr_buffer_load_lds(rsV, (LAS void*)(smem + SCR + w * 256), 4, vo, l * W_LAYER, 0, 0);
      else __builtin_amdgcn_raw_ptr_buffer_load_lds(rsU, (LAS void*)(smem + SCR + w * 256), 4, vo, l * W_LAYER, 0, 0);
    }
  }
  {
#pragma unroll
    for (int e = tid; e < kL * kD; e += 256) {
      const int l = e >> 9, d = e & 511;
      const int ww = d >> 7, q = (d >> 5) & 3, gg = (d >> 3) & 3, T = (d >> 2) & 1, j = d & 3;
      const float v = __builtin_bit_cast(float, (unsigned)bsg[e] << 16);
      *(LAS float*)(smem + BIAS + (((l * 4 + ww) * 16 + gg * 4 + j) * 8 + 2 * q + T) * 4) = v;
    }
  }

  int aoff[2], xoff[4];
#pragma unroll
  for (int s = 0; s < 2; ++s) aoff[s] = RING + w * 16384 + c16 * 128 + (((4 * s + g) ^ ((c16 >> 1) & 7)) << 4);
#pragma unroll
  for (int s = 0; s < 4; ++s) xoff[s] = XIMG + c16 * 256 + (((4 * s + g) ^ c16) << 4);
  const int hoff = HIMG + lane * 16;
  const int boff = BIAS + w * 512 + (g * 4 + (lane & 3)) * 32;

  auto ld = [&](int off) -> v4u { return *(LAS v4u*)(smem + off); };

  wait_vm<0>();
  barrier();

  v4u x0f[4][4];
#pragma unroll
  for (int q = 0; q < 4; ++q)
#pragma unroll
    for (int nb = 0; nb < 4; ++nb) x0f[q][nb] = ld(xoff[q] + w * 16384 + nb * 4096);

  f32x4 S[4][2][4];
#pragma unroll
  for (int q = 0; q < 4; ++q)
#pragma unroll
    for (int T = 0; T < 2; ++T)
#pragma unroll
      for (int nb = 0; nb < 4; ++nb) S[q][T][nb] = f32x4{1.f, 1.f, 1.f, 1.f};

  v4u afr[2][4];
#pragma unroll
  for (int f = 0; f < 4; ++f) afr[0][f] = ld(aoff[f & 1] + (f >> 1) * 2048);
  SBAR();

  for (int l = 0; l < kL; ++l) {
    v4u bfr[2][8];
    f32x4 H[2][4];
#pragma unroll
    for (int f = 0; f < 8; ++f) bfr[0][f] = ld(xoff[f >> 2] + (f & 3) * 4096);
#pragma unroll
    for (int t = 0; t < 8; ++t) {
      issue_unit(l, t + 3);
      wait_vm<8>();
#pragma unroll
      for (int f = 0; f < 4; ++f) afr[(t + 1) & 1][f] = ld(aoff[f & 1] + ((t + 1) & 3) * 4096 + (f >> 1) * 2048);
      if (t < 7) {
#pragma unroll
        for (int f = 0; f < 8; ++f) {
          const int s = 2 * (t + 1) + (f >> 2);
          bfr[(t + 1) & 1][f] = ld(xoff[s & 3] + (s >> 2) * 16384 + (f & 3) * 4096);
        }
      }
#pragma unroll
      for (int s = 0; s < 2; ++s)
#pragma unroll
        for (int T = 0; T < 2; ++T)
#pragma unroll
          for (int nb = 0; nb < 4; ++nb) {
            const f32x4 c = (t == 0 && s == 0) ? f32x4{0.f, 0.f, 0.f, 0.f} : H[T][nb];
            H[T][nb] = mfma16(afr[t & 1][2 * T + s], bfr[t & 1][4 * s + nb], c);
          }
      SBAR();
    }
#pragma unroll
    for (int nb = 0; nb < 4; ++nb) {
      v4u hp;
      hp.x = pk2(H[0][nb][0], H[0][nb][1]);
      hp.y = pk2(H[0][nb][2], H[0][nb][3]);
      hp.z = pk2(H[1][nb][0], H[1][nb][1]);
      hp.w = pk2(H[1][nb][2], H[1][nb][3]);
      *(LAS v4u*)(smem + hoff + (w * 4 + nb) * 1024) = hp;
    }
    {
      const f32x4 b0 = *(LAS f32x4*)(smem + boff + l * 2048);
      const f32x4 b1 = *(LAS f32x4*)(smem + boff + l * 2048 + 16);
#pragma unroll
      for (int q = 0; q < 4; ++q)
#pragma unroll
        for (int T = 0; T < 2; ++T) {
          const float bv = (q < 2) ? b0[2 * q + T] : b1[2 * (q - 2) + T];
#pragma unroll
          for (int nb = 0; nb < 4; ++nb) S[q][T][nb] = __builtin_amdgcn_mfma_f32_4x4x1f32(bv, 1.0f, S[q][T][nb], 0, 0, 0);
        }
    }
    barrier();
    v4u hfr[16];
#pragma unroll
    for (int f = 0; f < 16; ++f) hfr[f] = ld(hoff + f * 1024);
    SBAR();
#pragma unroll
    for (int t = 8; t < 16; ++t) {
      if (t + 3 < 16) issue_unit(l, t + 3);
      else issue_unit(l + 1, t + 3 - 16);
      wait_vm<8>();
#pragma unroll
      for (int f = 0; f < 4; ++f) afr[(t + 1) & 1][f] = ld(aoff[f & 1] + ((t + 1) & 3) * 4096 + (f >> 1) * 2048);
      const int q = (t - 8) >> 1, hf = (t - 8) & 1;
#pragma unroll
      for (int s = 0; s < 2; ++s)
#pragma unroll
        for (int T = 0; T < 2; ++T)
#pragma unroll
          for (int nb = 0; nb < 4; ++nb) S[q][T][nb] = mfma16(afr[t & 1][2 * T + s], hfr[(2 * hf + s) * 4 + nb], S[q][T][nb]);
      const int eq = (t - 10) >> 1;
      auto elem = [&](int qq, int nb) {
        const v4u xx = x0f[qq][nb];
        const f32x4 s0 = S[qq][0][nb], s1 = S[qq][1][nb];
        v4u o;
        o.x = pk2(bflo(xx.x) * s0[0], bfhi(xx.x) * s0[1]);
        o.y = pk2(bflo(xx.y) * s0[2], bfhi(xx.y) * s0[3]);
        o.z = pk2(bflo(xx.z) * s1[0], bfhi(xx.z) * s1[1]);
        o.w = pk2(bflo(xx.w) * s1[2], bfhi(xx.w) * s1[3]);
        *(LAS v4u*)(smem + xoff[qq] + w * 16384 + nb * 4096) = o;
      };
      if (t >= 10) {
        elem(eq, 2 * hf);
        elem(eq, 2 * hf + 1);
      }
      if (t == 15) {
        SBAR();
#pragma unroll
        for (int nb = 0; nb < 4; ++nb) elem(3, nb);
      }
      SBAR();
    }
    barrier();
  }

  unsigned char* ob = outg + (size_t)blk * (kM * kD * 2) + w * 256;
#pragma unroll
  for (int p = 0; p < 16; ++p) {
    const int row = 4 * p + g;
    const v4u v = ld(XIMG + w * 16384 + p * 1024 + lane * 16);
    *(v4u*)(ob + row * 1024 + ((c16 ^ (row & 15)) << 4)) = v;
  }
  wait_vm<0>();
}

}

extern "C" void kernel_launch(void* const* d_in, const int* in_sizes, int n_in, void* d_out, int out_size, void* d_ws,
                              size_t ws_size, hipStream_t stream) {
  const unsigned char* x0 = (const unsigned char*)d_in[0];
  const unsigned char* U = (const unsigned char*)d_in[1];
  const unsigned char* V = (const unsigned char*)d_in[2];
  const unsigned short* bs = (const unsigned short*)d_in[3];
  const int rows = in_sizes[0] / kD;
  (void)hipFuncSetAttribute((const void*)dcn_kernel, hipFuncAttributeMaxDynamicSharedMemorySize, LDS_BYTES);
  dcn_kernel<<<rows / kM, 256, LDS_BYTES, stream>>>(x0, U, V, bs, (unsigned char*)d_out);
}
```

```cpp
#define XFIRST 12
#define NOPF 1
#include <hip/hip_runtime.h>
#include <hip/hip_bf16.h>

namespace {

typedef unsigned v4u __attribute__((ext_vector_type(4)));
typedef unsigned v2u __attribute__((ext_vector_type(2)));
#define LAS __attribute__((address_space(3)))
#define SBAR() __builtin_amdgcn_sched_barrier(0)

constexpr int kD = 512, kR = 128, kL = 6;
constexpr int kM = 64;
constexpr int XIMG = 0;
constexpr int HIMG = 65536;
constexpr int RING = 81920;
constexpr int BIAS = 147456;
constexpr int FLAGS = 153600;
constexpr int SCR = 159744;
constexpr int LDS_BYTES = 160768;
constexpr int W_LAYER = kD * kR * 2;

__device__ __forceinline__ v4u make_srd(const unsigned char* p, unsigned bytes) {
  const unsigned long long a = (unsigned long long)p;
  v4u r;
  r.x = __builtin_amdgcn_readfirstlane((unsigned)a);
  r.y = __builtin_amdgcn_readfirstlane((unsigned)(a >> 32) & 0xffffu);
  r.z = __builtin_amdgcn_readfirstlane(bytes);
  r.w = 0x00020000u;
  return r;
}

__global__ void __launch_bounds__(256, 1)
dcn_kernel(const unsigned char* __restrict__ x0g, const unsigned char* __restrict__ Ug, const unsigned char* __restrict__ Vg,
           unsigned char* __restrict__ ws, const unsigned char* __restrict__ bsg, unsigned char* __restrict__ outg) {
  extern __shared__ __attribute__((aligned(16))) unsigned char smem_[];
  LAS unsigned char* smem = (LAS unsigned char*)smem_;
  const unsigned lbase = (unsigned)(size_t)smem;
  const int tid = threadIdx.x, lane = tid & 63;
  const int w = __builtin_amdgcn_readfirstlane(tid >> 6);
  const int blk = blockIdx.x;
  const int c16 = lane & 15, g = lane >> 4;
  const int wbase = w * 32768;

  const __amdgpu_buffer_rsrc_t rsB = __builtin_amdgcn_make_buffer_rsrc((void*)bsg, 0, kL * kD * 2, 0x00020000);

  const int j = lane >> 3;
  const int voX = j * 1024 + (((lane & 7) ^ j) << 4);
  {
    const __amdgpu_buffer_rsrc_t rsX = __builtin_amdgcn_make_buffer_rsrc((void*)(x0g + (size_t)blk * (kM * kD * 2)), 0, kM * kD * 2, 0x00020000);
#pragma unroll
    for (int p = 0; p < XFIRST; ++p)
      __builtin_amdgcn_raw_ptr_buffer_load_lds(rsX, (LAS void*)(smem + XIMG + w * 16384 + p * 1024), 16, voX, w * 256 + (p >> 3) * 128 + (p & 7) * 8192, 0, 2);
    SBAR();
  }
  const int chs = ((lane & 7) ^ (j & 6)) << 4;
  const int voV = (8 * (j >> 2) + 2 * (j & 3)) * 1024 + chs;
  const int voU = ((j & 3) + 16 * (j >> 2)) * 256 + chs;
  __builtin_amdgcn_raw_ptr_buffer_load_lds(rsB, (LAS void*)(smem + BIAS + w * 1536), 16, g * 1024 + c16 * 16, w * 256, 0, 0);
  __builtin_amdgcn_raw_ptr_buffer_load_lds(rsB, (LAS void*)(smem + BIAS + w * 1536 + 512), 16, g * 1024 + c16 * 16, w * 256 + 2048, 0, 0);
#pragma unroll
  for (int p = 0; p < 3; ++p) *(LAS v2u*)(smem + FLAGS + w * 1536 + p * 512 + lane * 8) = v2u{0u, 0u};
  const int part = (blk >> 3) & 31;
  const int pfV = ((part >> 2) * 128 + ((part >> 1) & 1) * 16384 + (part & 1) * 1024) |
                  (((part >> 3) * 8192 + ((part >> 2) & 1) * 128 + (part & 3) * 1024) << 16);

  const unsigned ring = lbase + RING + w * 16384;
  const int i = c16, ju = (i & 3) + 4 * (i >> 3);
  const unsigned aV0 = ring + 2048 * (i >> 3) + 128 * (i & 7) + ((g ^ (i & 6)) << 4);
  const unsigned aV1 = ring + 2048 * (i >> 3) + 128 * (i & 7) + (((4 + g) ^ (i & 6)) << 4);
  const unsigned aU0 = ring + 2048 * ((i >> 2) & 1) + 512 * (i >> 3) + 128 * (i & 3) + ((g ^ (ju & 6)) << 4);
  const unsigned aU1 = ring + 2048 * ((i >> 2) & 1) + 512 * (i >> 3) + 128 * (i & 3) + (((4 + g) ^ (ju & 6)) << 4);
  const unsigned xo0 = lbase + XIMG + c16 * 128 + ((g ^ (c16 & 7)) << 4);
  const unsigned xo1 = lbase + XIMG + c16 * 128 + (((4 + g) ^ (c16 & 7)) << 4);
  const unsigned hoff = lbase + HIMG + lane * 16;
  unsigned boff = lbase + BIAS + w * 1536 + (8 * (c16 >> 2) + (lane & 3)) * 2;
  unsigned fwo = lbase + FLAGS + g * 1536 + (8 * (c16 >> 2) + w) * 2;
  const v4u sV0 = make_srd(Vg + wbase, W_LAYER - wbase);
  const v4u sU = make_srd(Ug + wbase, W_LAYER - wbase);
  const unsigned hc = lbase + HIMG;
  const v4u sW = make_srd(ws + 65536 + w * 65536 - hc, kL * 262144 - w * 65536 + hc);
  const int cw = blk >> 6, tt = (blk >> 2) & 15, TT = (blk >> 1) & 1, ss = blk & 1;
  const int uu = ((tt & 3) << 1) | ((tt >> 2) & 1);
  const int srcV = (32 * cw + TT) * 1024 + (8 * uu + 4 * ss) * 16 + 1;
  const int srcU = (128 * cw + 32 * ((tt >> 1) & 3) + 4 * TT) * 256 + (8 * (tt & 1) + 4 * ss) * 16;
  const int srcoff = tt < 8 ? srcV : srcU;
  const v4u sS = make_srd(tt < 8 ? Vg : Ug, kL * W_LAYER);
  const v4u sC = make_srd(ws, 8192);
  const v4u sE = make_srd(ws, (blk == 0 && w == 0) ? 8 : 0);
  const v4u sF = make_srd(ws + 4096 + blk * 8, w == 0 ? 8 : 0);
  const v4u sX = make_srd(x0g + (size_t)blk * (kM * kD * 2) + w * 256, kM * kD * 2 - w * 256);
  const v4u sO = make_srd(outg + (size_t)blk * (kM * kD * 2) + w * 256, kM * kD * 2 - w * 256);

  asm volatile(
    "s_mov_b32 s25, 0xffff0000\n"
    "s_mov_b32 s20, 0\n"
    "s_mov_b32 s21, 262144\n"
    "s_mov_b32 s24, 5\n"
    "v_mbcnt_lo_u32_b32 v204, -1, 0\n"
    "v_mbcnt_hi_u32_b32 v204, -1, v204\n"
    "s_and_b32 s28, %18, 1\n"
    "v_and_b32 v205, 3, v204\n"
    "v_lshlrev_b32 v205, s28, v205\n"
    "v_bfe_u32 v206, v204, 2, 2\n"
    "v_lshl_add_u32 v205, v206, 3, v205\n"
    "s_lshl_b32 s28, s28, 1\n"
    "s_add_u32 s28, s28, 8\n"
    "v_lshlrev_b32 v205, s28, v205\n"
    "v_and_b32 v206, 48, v204\n"
    "v_add_u32 v205, v205, v206\n"
    "v_mov_b32 v0, 1.0\n"
    "v_mov_b32 v1, 1.0\n"
    "v_mov_b32 v2, 1.0\n"
    "v_mov_b32 v3, 1.0\n"
    "v_mov_b32 v4, 1.0\n"
    "v_mov_b32 v5, 1.0\n"
    "v_mov_b32 v6, 1.0\n"
    "v_mov_b32 v7, 1.0\n"
    "v_mov_b32 v8, 1.0\n"
    "v_mov_b32 v9, 1.0\n"
    "v_mov_b32 v10, 1.0\n"
    "v_mov_b32 v11, 1.0\n"
    "v_mov_b32 v12, 1.0\n"
    "v_mov_b32 v13, 1.0\n"
    "v_mov_b32 v14, 1.0\n"
    "v_mov_b32 v15, 1.0\n"
    "v_mov_b32 v16, 1.0\n"
    "v_mov_b32 v17, 1.0\n"
    "v_mov_b32 v18, 1.0\n"
    "v_mov_b32 v19, 1.0\n"
    "v_mov_b32 v20, 1.0\n"
    "v_mov_b32 v21, 1.0\n"
    "v_mov_b32 v22, 1.0\n"
    "v_mov_b32 v23, 1.0\n"
    "v_mov_b32 v24, 1.0\n"
    "v_mov_b32 v25, 1.0\n"
    "v_mov_b32 v26, 1.0\n"
    "v_mov_b32 v27, 1.0\n"
    "v_mov_b32 v28, 1.0\n"
    "v_mov_b32 v29, 1.0\n"
    "v_mov_b32 v30, 1.0\n"
    "v_mov_b32 v31, 1.0\n"
    "v_mov_b32 v32, 1.0\n"
    "v_mov_b32 v33, 1.0\n"
    "v_mov_b32 v34, 1.0\n"
    "v_mov_b32 v35, 1.0\n"
    "v_mov_b32 v36, 1.0\n"
    "v_mov_b32 v37, 1.0\n"
    "v_mov_b32 v38, 1.0\n"
    "v_mov_b32 v39, 1.0\n"
    "v_mov_b32 v40, 1.0\n"
    "v_mov_b32 v41, 1.0\n"
    "v_mov_b32 v42, 1.0\n"
    "v_mov_b32 v43, 1.0\n"
    "v_mov_b32 v44, 1.0\n"
    "v_mov_b32 v45, 1.0\n"
    "v_mov_b32 v46, 1.0\n"
    "v_mov_b32 v47, 1.0\n"
    "v_mov_b32 v48, 1.0\n"
    "v_mov_b32 v49, 1.0\n"
    "v_mov_b32 v50, 1.0\n"
    "v_mov_b32 v51, 1.0\n"
    "v_mov_b32 v52, 1.0\n"
    "v_mov_b32 v53, 1.0\n"
    "v_mov_b32 v54, 1.0\n"
    "v_mov_b32 v55, 1.0\n"
    "v_mov_b32 v56, 1.0\n"
    "v_mov_b32 v57, 1.0\n"
    "v_mov_b32 v58, 1.0\n"
    "v_mov_b32 v59, 1.0\n"
    "v_mov_b32 v60, 1.0\n"
    "v_mov_b32 v61, 1.0\n"
    "v_mov_b32 v62, 1.0\n"
    "v_mov_b32 v63, 1.0\n"
    "v_mov_b32 v64, 1.0\n"
    "v_mov_b32 v65, 1.0\n"
    "v_mov_b32 v66, 1.0\n"
    "v_mov_b32 v67, 1.0\n"
    "v_mov_b32 v68, 1.0\n"
    "v_mov_b32 v69, 1.0\n"
    "v_mov_b32 v70, 1.0\n"
    "v_mov_b32 v71, 1.0\n"
    "v_mov_b32 v72, 1.0\n"
    "v_mov_b32 v73, 1.0\n"
    "v_mov_b32 v74, 1.0\n"
    "v_mov_b32 v75, 1.0\n"
    "v_mov_b32 v76, 1.0\n"
    "v_mov_b32 v77, 1.0\n"
    "v_mov_b32 v78, 1.0\n"
    "v_mov_b32 v79, 1.0\n"
    "v_mov_b32 v80, 1.0\n"
    "v_mov_b32 v81, 1.0\n"
    "v_mov_b32 v82, 1.0\n"
    "v_mov_b32 v83, 1.0\n"
    "v_mov_b32 v84, 1.0\n"
    "v_mov_b32 v85, 1.0\n"
    "v_mov_b32 v86, 1.0\n"
    "v_mov_b32 v87, 1.0\n"
    "v_mov_b32 v88, 1.0\n"
    "v_mov_b32 v89, 1.0\n"
    "v_mov_b32 v90, 1.0\n"
    "v_mov_b32 v91, 1.0\n"
    "v_mov_b32 v92, 1.0\n"
    "v_mov_b32 v93, 1.0\n"
    "v_mov_b32 v94, 1.0\n"
    "v_mov_b32 v95, 1.0\n"
    "v_mov_b32 v96, 1.0\n"
    "v_mov_b32 v97, 1.0\n"
    "v_mov_b32 v98, 1.0\n"
    "v_mov_b32 v99, 1.0\n"
    "v_mov_b32 v100, 1.0\n"
    "v_mov_b32 v101, 1.0\n"
    "v_mov_b32 v102, 1.0\n"
    "v_mov_b32 v103, 1.0\n"
    "v_mov_b32 v104, 1.0\n"
    "v_mov_b32 v105, 1.0\n"
    "v_mov_b32 v106, 1.0\n"
    "v_mov_b32 v107, 1.0\n"
    "v_mov_b32 v108, 1.0\n"
    "v_mov_b32 v109, 1.0\n"
    "v_mov_b32 v110, 1.0\n"
    "v_mov_b32 v111, 1.0\n"
    "v_mov_b32 v112, 1.0\n"
    "v_mov_b32 v113, 1.0\n"
    "v_mov_b32 v114, 1.0\n"
    "v_mov_b32 v115, 1.0\n"
    "v_mov_b32 v116, 1.0\n"
    "v_mov_b32 v117, 1.0\n"
    "v_mov_b32 v118, 1.0\n"
    "v_mov_b32 v119, 1.0\n"
    "v_mov_b32 v120, 1.0\n"
    "v_mov_b32 v121, 1.0\n"
    "v_mov_b32 v122, 1.0\n"
    "v_mov_b32 v123, 1.0\n"
    "v_mov_b32 v124, 1.0\n"
    "v_mov_b32 v125, 1.0\n"
    "v_mov_b32 v126, 1.0\n"
    "v_mov_b32 v127, 1.0\n"
    "v_mov_b32 v239, 0x3f80\n"
    "v_accvgpr_write_b32 a248, v239\n"
    "v_accvgpr_write_b32 a249, 0\n"
    "v_accvgpr_write_b32 a240, 0\n"
    "v_accvgpr_write_b32 a241, 0\n"
    "v_accvgpr_write_b32 a242, 0\n"
    "v_accvgpr_write_b32 a243, 0\n"
    "v_accvgpr_write_b32 a244, 0\n"
    "v_accvgpr_write_b32 a245, 0\n"
    "v_accvgpr_write_b32 a246, 0\n"
    "v_accvgpr_write_b32 a247, 0\n"
    "v_add_u32 v240, %4, %8\n"
    "v_add_u32 v241, %4, %9\n"
    "v_mbcnt_lo_u32_b32 v226, -1, 0\n"
    "v_mbcnt_hi_u32_b32 v226, -1, v226\n"
    "v_lshlrev_b32 v224, 7, v226\n"
    "v_lshlrev_b32 v225, 6, v226\n"
    "v_add_u32 v225, %5, v225\n"
    "v_lshlrev_b32 v225, 1, v225\n"
    "v_min_u32 v225, .Ldcn_end%=-.Ldcn_pc%=-4, v225\n"
    "s_getpc_b64 s[26:27]\n"
    ".Ldcn_pc%=:\n"
    "s_add_u32 m0, %13, 12288\n"
    "s_mov_b32 s28, 32896\n"
    "buffer_load_dwordx4 %11, %12, s28 offen nt lds\n"
    "s_add_u32 m0, %13, 13312\n"
    "s_mov_b32 s28, 41088\n"
    "buffer_load_dwordx4 %11, %12, s28 offen nt lds\n"
    "s_add_u32 m0, %13, 14336\n"
    "s_mov_b32 s28, 49280\n"
    "buffer_load_dwordx4 %11, %12, s28 offen nt lds\n"
    "s_add_u32 m0, %13, 15360\n"
    "s_mov_b32 s28, 57472\n"
    "buffer_load_dwordx4 %11, %12, s28 offen nt lds\n"
    "s_mov_b32 s29, 0\n"
    "s_add_u32 m0, %23, 0\n"
    "s_nop 0\n"
    "buffer_load_dwordx4 %24, %21, s29 offen offset:0 lds\n"
    "buffer_load_dwordx4 %24, %21, s29 offen offset:1024 lds\n"
    "s_mov_b32 s29, 16384\n"
    "s_add_u32 m0, %23, 2048\n"
    "s_nop 0\n"
    "buffer_load_dwordx4 %24, %21, s29 offen offset:0 lds\n"
    "buffer_load_dwordx4 %24, %21, s29 offen offset:1024 lds\n"
    "s_mov_b32 s29, 256\n"
    "s_add_u32 m0, %23, 4096\n"
    "s_nop 0\n"
    "buffer_load_dwordx4 %24, %21, s29 offen offset:0 lds\n"
    "buffer_load_dwordx4 %24, %21, s29 offen offset:1024 lds\n"
    "s_mov_b32 s29, 16640\n"
    "s_add_u32 m0, %23, 6144\n"
    "s_nop 0\n"
    "buffer_load_dwordx4 %24, %21, s29 offen offset:0 lds\n"
    "buffer_load_dwordx4 %24, %21, s29 offen offset:1024 lds\n"
    "s_mov_b32 s29, 512\n"
    "s_add_u32 m0, %23, 8192\n"
    "s_nop 0\n"
    "buffer_load_dwordx4 %24, %21, s29 offen offset:0 lds\n"
    "buffer_load_dwordx4 %24, %21, s29 offen offset:1024 lds\n"
    "s_mov_b32 s29, 16896\n"
    "s_add_u32 m0, %23, 10240\n"
    "s_nop 0\n"
    "buffer_load_dwordx4 %24, %21, s29 offen offset:0 lds\n"
    "buffer_load_dwordx4 %24, %21, s29 offen offset:1024 lds\n"
    "s_mov_b32 s29, 768\n"
    "s_add_u32 m0, %23, 12288\n"
    "s_nop 0\n"
    "buffer_load_dwordx4 %24, %21, s29 offen offset:0 lds\n"
    "buffer_load_dwordx4 %24, %21, s29 offen offset:1024 lds\n"
    "s_mov_b32 s29, 17152\n"
    "s_add_u32 m0, %23, 14336\n"
    "s_nop 0\n"
    "buffer_load_dwordx4 %24, %21, s29 offen offset:0 lds\n"
    "buffer_load_dwordx4 %24, %21, s29 offen offset:1024 lds\n"
    "s_mov_b32 m0, %7\n"
    "s_mov_b32 exec_lo, 0x01010101\n"
    "s_mov_b32 exec_hi, 0x01010101\n"
    "s_and_b32 s29, %6, 0xffff\n"
    "buffer_load_dword %24, %21, s29 offen lds\n"
    "s_lshr_b32 s29, %6, 16\n"
    "buffer_load_dword %25, %22, s29 offen lds\n"
    "s_mov_b64 exec, -1\n"
    "s_nop 3\n"
    "s_mov_b32 m0, %7\n"
    "s_nop 0\n"
    "global_load_lds_dword v225, s[26:27]\n"
    "s_andn2_b32 s29, %18, 15\n"
    "buffer_load_dwordx2 v[236:237], off, %15, 0 sc1\n"
    "buffer_load_dwordx4 v[228:231], v205, %14, s29 offen nt\n"
    "s_add_u32 s29, s29, 524288\n"
    "buffer_load_dwordx4 v[232:235], v205, %14, s29 offen nt\n"
    "s_waitcnt vmcnt(32)\n"
    "s_waitcnt lgkmcnt(0)\n"
    "s_barrier\n"
    "ds_read_b128 a[192:195], %8 offset:0\n"
    "ds_read_b128 a[196:199], %9 offset:0\n"
    "ds_read_b128 a[200:203], %8 offset:16384\n"
    "ds_read_b128 a[204:207], %9 offset:16384\n"
    "s_waitcnt vmcnt(18)\n"
    "ds_read_b128 a[0:3], %26 offset:0\n"
    "ds_read_b128 a[8:11], %26 offset:1024\n"
    "ds_read_b128 a[4:7], %27 offset:0\n"
    "ds_read_b128 a[12:15], %27 offset:1024\n"
    "s_waitcnt vmcnt(14)\n"
    "ds_read_b128 a[16:19], %26 offset:4096\n"
    "ds_read_b128 a[24:27], %26 offset:5120\n"
    "ds_read_b128 a[20:23], %27 offset:4096\n"
    "ds_read_b128 a[28:31], %27 offset:5120\n"
    "ds_read_b128 a[208:211], %8 offset:2048\n"
    "ds_read_b128 a[212:215], %9 offset:2048\n"
    "ds_read_b128 a[216:219], %8 offset:18432\n"
    "ds_read_b128 a[220:223], %9 offset:18432\n"
    "ds_read_b128 v[128:131], v240 offset:0\n"
    "ds_read_b128 v[132:135], v240 offset:2048\n"
    "ds_read_b128 v[136:139], v240 offset:4096\n"
    "ds_read_b128 v[140:143], v240 offset:6144\n"
    "ds_read_b128 v[144:147], v241 offset:0\n"
    "ds_read_b128 v[148:151], v241 offset:2048\n"
    "ds_read_b128 v[152:155], v241 offset:4096\n"
    "ds_read_b128 v[156:159], v241 offset:6144\n"
    "s_waitcnt lgkmcnt(15)\n"
    "s_mov_b32 s29, 128\n"
    "s_add_u32 m0, %23, 0\n"
    "s_nop 0\n"
    "buffer_load_dwordx4 %24, %21, s29 offen offset:0 lds\n"
    "buffer_load_dwordx4 %24, %21, s29 offen offset:1024 lds\n"
    "s_mov_b32 s29, 16512\n"
    "s_add_u32 m0, %23, 2048\n"
    "s_nop 0\n"
    "buffer_load_dwordx4 %24, %21, s29 offen offset:0 lds\n"
    "buffer_load_dwordx4 %24, %21, s29 offen offset:1024 lds\n"
    "v_mfma_f32_16x16x32_bf16 v[192:195], a[0:3], a[192:195], 0\n"
    "s_add_u32 m0, %23, 4096\n"
    "s_mov_b32 s29, 384\n"
    "v_mfma_f32_16x16x32_bf16 v[192:195], a[4:7], a[196:199], v[192:195]\n"
    "s_waitcnt vmcnt(14) lgkmcnt(11)\n"
    "buffer_load_dwordx4 %24, %21, s29 offen offset:0 lds\n"
    "ds_read_b128 a[32:35], %26 offset:8192\n"
    "v_mfma_f32_16x16x32_bf16 v[192:195], a[16:19], a[200:203], v[192:195]\n"
    "ds_read_b128 a[40:43], %26 offset:9216\n"
    "ds_read_b128 a[224:227], %8 offset:4096\n"
    "v_mfma_f32_16x16x32_bf16 v[192:195], a[20:23], a[204:207], v[192:195]\n"
    "ds_read_b128 a[228:231], %9 offset:4096\n"
    "ds_read_b128 a[232:235], %8 offset:20480\n"
    "v_mfma_f32_16x16x32_bf16 v[208:211], a[8:11], a[192:195], 0\n"
    "ds_read_b128 a[236:239], %9 offset:20480\n"
    "v_mfma_f32_16x16x32_bf16 v[208:211], a[12:15], a[196:199], v[208:211]\n"
    "buffer_load_dwordx4 %24, %21, s29 offen offset:1024 lds\n"
    "v_mfma_f32_16x16x32_bf16 v[208:211], a[24:27], a[200:203], v[208:211]\n"
    "v_mfma_f32_16x16x32_bf16 v[208:211], a[28:31], a[204:207], v[208:211]\n"
    "s_waitcnt lgkmcnt(6)\n"
    "v_mfma_f32_16x16x32_bf16 v[196:199], a[0:3], a[208:211], 0\n"
    "s_add_u32 m0, %23, 6144\n"
    "ds_read_b128 a[36:39], %27 offset:8192\n"
    "ds_read_b128 a[44:47], %27 offset:9216\n"
    "s_mov_b32 s29, 16768\n"
    "v_mfma_f32_16x16x32_bf16 v[196:199], a[4:7], a[212:215], v[196:199]\n"
    "buffer_load_dwordx4 %24, %21, s29 offen offset:0 lds\n"
    "ds_read_b128 a[192:195], %8 offset:6144\n"
    "v_mfma_f32_16x16x32_bf16 v[196:199], a[16:19], a[216:219], v[196:199]\n"
    "ds_read_b128 a[196:199], %9 offset:6144\n"
    "ds_read_b128 a[200:203], %8 offset:22528\n"
    "v_mfma_f32_16x16x32_bf16 v[196:199], a[20:23], a[220:223], v[196:199]\n"
    "ds_read_b128 a[204:207], %9 offset:22528\n"
    "v_mfma_f32_16x16x32_bf16 v[212:215], a[8:11], a[208:211], 0\n"
    "v_mfma_f32_16x16x32_bf16 v[212:215], a[12:15], a[212:215], v[212:215]\n"
    "buffer_load_dwordx4 %24, %21, s29 offen offset:1024 lds\n"
    "v_mfma_f32_16x16x32_bf16 v[212:215], a[24:27], a[216:219], v[212:215]\n"
    "v_mfma_f32_16x16x32_bf16 v[212:215], a[28:31], a[220:223], v[212:215]\n"
    "s_waitcnt lgkmcnt(7)\n"
    "v_mfma_f32_16x16x32_bf16 v[200:203], a[0:3], a[224:227], 0\n"
    "s_add_u32 m0, %23, 8192\n"
    "s_mov_b32 s29, 640\n"
    "v_mfma_f32_16x16x32_bf16 v[200:203], a[4:7], a[228:231], v[200:203]\n"
    "s_waitcnt vmcnt(14) lgkmcnt(4)\n"
    "buffer_load_dwordx4 %24, %21, s29 offen offset:0 lds\n"
    "ds_read_b128 a[48:51], %26 offset:12288\n"
    "v_mfma_f32_16x16x32_bf16 v[200:203], a[16:19], a[232:235], v[200:203]\n"
    "ds_read_b128 a[56:59], %26 offset:13312\n"
    "ds_read_b128 a[208:211], %8 offset:32768\n"
    "v_mfma_f32_16x16x32_bf16 v[200:203], a[20:23], a[236:239], v[200:203]\n"
    "ds_read_b128 a[212:215], %9 offset:32768\n"
    "ds_read_b128 a[216:219], %8 offset:49152\n"
    "v_mfma_f32_16x16x32_bf16 v[216:219], a[8:11], a[224:227], 0\n"
    "ds_read_b128 a[220:223], %9 offset:49152\n"
    "v_mfma_f32_16x16x32_bf16 v[216:219], a[12:15], a[228:231], v[216:219]\n"
    "buffer_load_dwordx4 %24, %21, s29 offen offset:1024 lds\n"
    "v_mfma_f32_16x16x32_bf16 v[216:219], a[24:27], a[232:235], v[216:219]\n"
    "v_mfma_f32_16x16x32_bf16 v[216:219], a[28:31], a[236:239], v[216:219]\n"
    "s_waitcnt lgkmcnt(6)\n"
    "v_mfma_f32_16x16x32_bf16 v[204:207], a[0:3], a[192:195], 0\n"
    "s_add_u32 m0, %23, 10240\n"
    "ds_read_b128 a[52:55], %27 offset:12288\n"
    "ds_read_b128 a[60:63], %27 offset:13312\n"
    "s_mov_b32 s29, 17024\n"
    "v_mfma_f32_16x16x32_bf16 v[204:207], a[4:7], a[196:199], v[204:207]\n"
    "buffer_load_dwordx4 %24, %21, s29 offen offset:0 lds\n"
    "ds_read_b128 a[224:227], %8 offset:34816\n"
    "v_mfma_f32_16x16x32_bf16 v[204:207], a[16:19], a[200:203], v[204:207]\n"
    "ds_read_b128 a[228:231], %9 offset:34816\n"
    "ds_read_b128 a[232:235], %8 offset:51200\n"
    "v_mfma_f32_16x16x32_bf16 v[204:207], a[20:23], a[204:207], v[204:207]\n"
    "ds_read_b128 a[236:239], %9 offset:51200\n"
    "v_mfma_f32_16x16x32_bf16 v[220:223], a[8:11], a[192:195], 0\n"
    "v_mfma_f32_16x16x32_bf16 v[220:223], a[12:15], a[196:199], v[220:223]\n"
    "buffer_load_dwordx4 %24, %21, s29 offen offset:1024 lds\n"
    "v_mfma_f32_16x16x32_bf16 v[220:223], a[24:27], a[200:203], v[220:223]\n"
    "v_mfma_f32_16x16x32_bf16 v[220:223], a[28:31], a[204:207], v[220:223]\n"
    "ds_write_b16 %1, a248 offset:8\n"
    "ds_read_b128 v[160:163], v240 offset:8192\n"
    "ds_read_b128 v[164:167], v240 offset:10240\n"
    "ds_read_b128 v[168:171], v240 offset:12288\n"
    "ds_read_b128 v[172:175], v240 offset:14336\n"
    "ds_read_b128 v[176:179], v241 offset:8192\n"
    "ds_read_b128 v[180:183], v241 offset:10240\n"
    "ds_read_b128 v[184:187], v241 offset:12288\n"
    "ds_read_b128 v[188:191], v241 offset:14336\n"
    "s_waitcnt vmcnt(12)\n"
    "buffer_store_dwordx4 v[228:231], %10, %2, %19 offen sc1\n"
    "s_add_u32 s28, %19, 1048576\n"
    "buffer_store_dwordx4 v[232:235], %10, %2, s28 offen sc1\n"
    "v_add_co_u32 v236, vcc, 0x7f4a7c15, v236\n"
    "v_mov_b32 v238, 0x9e3779b9\n"
    "s_nop 1\n"
    "v_addc_co_u32 v237, vcc, v238, v237, vcc\n"
    "v_accvgpr_write_b32 a252, v236\n"
    "v_accvgpr_write_b32 a253, v237\n"
    "s_waitcnt lgkmcnt(10)\n"
    "v_mfma_f32_16x16x32_bf16 v[192:195], a[32:35], a[208:211], v[192:195]\n"
    "s_add_u32 m0, %23, 12288\n"
    "s_mov_b32 s29, 896\n"
    "v_mfma_f32_16x16x32_bf16 v[192:195], a[36:39], a[212:215], v[192:195]\n"
    "s_waitcnt vmcnt(10)\n"
    "buffer_load_dwordx4 %24, %21, s29 offen offset:0 lds\n"
    "ds_read_b128 a[64:67], %26 offset:0\n"
    "v_mfma_f32_16x16x32_bf16 v[192:195], a[48:51], a[216:219], v[192:195]\n"
    "ds_read_b128 a[72:75], %26 offset:1024\n"
    "ds_read_b128 a[192:195], %8 offset:36864\n"
    "v_mfma_f32_16x16x32_bf16 v[192:195], a[52:55], a[220:223], v[192:195]\n"
    "ds_read_b128 a[196:199], %9 offset:36864\n"
    "ds_read_b128 a[200:203], %8 offset:53248\n"
    "v_mfma_f32_16x16x32_bf16 v[208:211], a[40:43], a[208:211], v[208:211]\n"
    "ds_read_b128 a[204:207], %9 offset:53248\n"
    "v_mfma_f32_16x16x32_bf16 v[208:211], a[44:47], a[212:215], v[208:211]\n"
    "buffer_load_dwordx4 %24, %21, s29 offen offset:1024 lds\n"
    "v_mfma_f32_16x16x32_bf16 v[208:211], a[56:59], a[216:219], v[208:211]\n"
    "v_mfma_f32_16x16x32_bf16 v[208:211], a[60:63], a[220:223], v[208:211]\n"
    "v_mfma_f32_16x16x32_bf16 v[196:199], a[32:35], a[224:227], v[196:199]\n"
    "s_add_u32 m0, %23, 14336\n"
    "ds_read_u16 v242, %0 offset:6152\n"
    "ds_read_b128 a[68:71], %27 offset:0\n"
    "s_mov_b32 s29, 17280\n"
    "v_mfma_f32_16x16x32_bf16 v[196:199], a[36:39], a[228:231], v[196:199]\n"
    "buffer_load_dwordx4 %24, %21, s29 offen offset:0 lds\n"
    "ds_read_b128 a[76:79], %27 offset:1024\n"
    "v_mfma_f32_16x16x32_bf16 v[196:199], a[48:51], a[232:235], v[196:199]\n"
    "s_waitcnt lgkmcnt(9)\n"
    "ds_read_b128 a[208:211], %8 offset:38912\n"
    "ds_read_b128 a[212:215], %9 offset:38912\n"
    "v_mfma_f32_16x16x32_bf16 v[196:199], a[52:55], a[236:239], v[196:199]\n"
    "ds_read_b128 a[216:219], %8 offset:55296\n"
    "ds_read_b128 a[220:223], %9 offset:55296\n"
    "s_waitcnt lgkmcnt(6)\n"
    "v_cmp_eq_u32 vcc, 0, v242\n"
    "v_mfma_f32_16x16x32_bf16 v[212:215], a[40:43], a[224:227], v[212:215]\n"
    "v_mfma_f32_16x16x32_bf16 v[212:215], a[44:47], a[228:231], v[212:215]\n"
    "s_cbranch_vccnz .Lslow1f_%=\n"
    ".Lchk1f_%=:\n"
    "buffer_load_dwordx4 %24, %21, s29 offen offset:1024 lds\n"
    "v_mfma_f32_16x16x32_bf16 v[212:215], a[56:59], a[232:235], v[212:215]\n"
    "v_mfma_f32_16x16x32_bf16 v[212:215], a[60:63], a[236:239], v[212:215]\n"
    "v_mfma_f32_16x16x32_bf16 v[200:203], a[32:35], a[192:195], v[200:203]\n"
    "s_add_u32 m0, %23, 0\n"
    "s_mov_b32 s29, 0\n"
    "v_mfma_f32_16x16x32_bf16 v[200:203], a[36:39], a[196:199], v[200:203]\n"
    "s_waitcnt vmcnt(10) lgkmcnt(4)\n"
    "buffer_load_dwordx4 %25, %22, s29 offen offset:0 lds\n"
    "ds_read_b128 a[80:83], %26 offset:4096\n"
    "v_mfma_f32_16x16x32_bf16 v[200:203], a[48:51], a[200:203], v[200:203]\n"
    "ds_read_b128 a[88:91], %26 offset:5120\n"
    "ds_read_b128 a[224:227], %8 offset:8192\n"
    "v_mfma_f32_16x16x32_bf16 v[200:203], a[52:55], a[204:207], v[200:203]\n"
    "ds_read_b128 a[228:231], %9 offset:8192\n"
    "ds_read_b128 a[232:235], %8 offset:24576\n"
    "v_mfma_f32_16x16x32_bf16 v[216:219], a[40:43], a[192:195], v[216:219]\n"
    "ds_read_b128 a[236:239], %9 offset:24576\n"
    "v_mfma_f32_16x16x32_bf16 v[216:219], a[44:47], a[196:199], v[216:219]\n"
    "buffer_load_dwordx4 %25, %22, s29 offen offset:1024 lds\n"
    "v_mfma_f32_16x16x32_bf16 v[216:219], a[56:59], a[200:203], v[216:219]\n"
    "v_mfma_f32_16x16x32_bf16 v[216:219], a[60:63], a[204:207], v[216:219]\n"
    "s_waitcnt lgkmcnt(6)\n"
    "v_mfma_f32_16x16x32_bf16 v[204:207], a[32:35], a[208:211], v[204:207]\n"
    "ds_read_b128 a[84:87], %27 offset:4096\n"
    "ds_read_b128 a[92:95], %27 offset:5120\n"
    "v_mfma_f32_16x16x32_bf16 v[204:207], a[36:39], a[212:215], v[204:207]\n"
    "buffer_load_dwordx4 %25, %22, s29 offen offset:2048 lds\n"
    "ds_read_b128 a[192:195], %8 offset:10240\n"
    "v_mfma_f32_16x16x32_bf16 v[204:207], a[48:51], a[216:219], v[204:207]\n"
    "ds_read_b128 a[196:199], %9 offset:10240\n"
    "ds_read_b128 a[200:203], %8 offset:26624\n"
    "v_mfma_f32_16x16x32_bf16 v[204:207], a[52:55], a[220:223], v[204:207]\n"
    "ds_read_b128 a[204:207], %9 offset:26624\n"
    "v_mfma_f32_16x16x32_bf16 v[220:223], a[40:43], a[208:211], v[220:223]\n"
    "v_mfma_f32_16x16x32_bf16 v[220:223], a[44:47], a[212:215], v[220:223]\n"
    "buffer_load_dwordx4 %25, %22, s29 offen offset:3072 lds\n"
    "v_mfma_f32_16x16x32_bf16 v[220:223], a[56:59], a[216:219], v[220:223]\n"
    "v_mfma_f32_16x16x32_bf16 v[220:223], a[60:63], a[220:223], v[220:223]\n"
    "s_waitcnt lgkmcnt(6)\n"
    "v_mfma_f32_16x16x32_bf16 v[192:195], a[64:67], a[224:227], v[192:195]\n"
    "s_add_u32 m0, %23, 4096\n"
    "s_mov_b32 s29, 128\n"
    "v_mfma_f32_16x16x32_bf16 v[192:195], a[68:71], a[228:231], v[192:195]\n"
    "s_waitcnt vmcnt(10) lgkmcnt(4)\n"
    "buffer_load_dwordx4 %25, %22, s29 offen offset:0 lds\n"
    "ds_read_b128 a[96:99], %26 offset:8192\n"
    "v_mfma_f32_16x16x32_bf16 v[192:195], a[80:83], a[232:235], v[192:195]\n"
    "ds_read_b128 a[104:107], %26 offset:9216\n"
    "ds_read_b128 a[208:211], %8 offset:12288\n"
    "v_mfma_f32_16x16x32_bf16 v[192:195], a[84:87], a[236:239], v[192:195]\n"
    "ds_read_b128 a[212:215], %9 offset:12288\n"
    "ds_read_b128 a[216:219], %8 offset:28672\n"
    "v_mfma_f32_16x16x32_bf16 v[208:211], a[72:75], a[224:227], v[208:211]\n"
    "ds_read_b128 a[220:223], %9 offset:28672\n"
    "ds_read_u16 a240, %0 offset:0\n"
    "v_mfma_f32_16x16x32_bf16 v[208:211], a[76:79], a[228:231], v[208:211]\n"
    "buffer_load_dwordx4 %25, %22, s29 offen offset:1024 lds\n"
    "ds_read_u16 a242, %0 offset:8\n"
    "v_mfma_f32_16x16x32_bf16 v[208:211], a[88:91], a[232:235], v[208:211]\n"
    "v_mfma_f32_16x16x32_bf16 v[208:211], a[92:95], a[236:239], v[208:211]\n"
    "s_waitcnt lgkmcnt(8)\n"
    "v_mfma_f32_16x16x32_bf16 v[196:199], a[64:67], a[192:195], v[196:199]\n"
    "ds_read_b128 a[100:103], %27 offset:8192\n"
    "ds_read_b128 a[108:111], %27 offset:9216\n"
    "v_mfma_f32_16x16x32_bf16 v[196:199], a[68:71], a[196:199], v[196:199]\n"
    "buffer_load_dwordx4 %25, %22, s29 offen offset:2048 lds\n"
    "ds_read_b128 a[224:227], %8 offset:14336\n"
    "v_mfma_f32_16x16x32_bf16 v[196:199], a[80:83], a[200:203], v[196:199]\n"
    "ds_read_b128 a[228:231], %9 offset:14336\n"
    "ds_read_b128 a[232:235], %8 offset:30720\n"
    "v_mfma_f32_16x16x32_bf16 v[196:199], a[84:87], a[204:207], v[196:199]\n"
    "ds_read_b128 a[236:239], %9 offset:30720\n"
    "ds_read_u16 a244, %0 offset:64\n"
    "v_mfma_f32_16x16x32_bf16 v[212:215], a[72:75], a[192:195], v[212:215]\n"
    "ds_read_u16 a246, %0 offset:72\n"
    "v_mfma_f32_16x16x32_bf16 v[212:215], a[76:79], a[196:199], v[212:215]\n"
    "buffer_load_dwordx4 %25, %22, s29 offen offset:3072 lds\n"
    "v_mfma_f32_16x16x32_bf16 v[212:215], a[88:91], a[200:203], v[212:215]\n"
    "v_mfma_f32_16x16x32_bf16 v[212:215], a[92:95], a[204:207], v[212:215]\n"
    "s_waitcnt vmcnt(12) lgkmcnt(9)\n"
    "s_waitcnt vmcnt(0)\n"
    "ds_write_b16 %1, a248 offset:192\n"
    "v_mfma_f32_16x16x32_bf16 v[200:203], a[64:67], a[208:211], v[200:203]\n"
    "s_add_u32 m0, %23, 8192\n"
    "s_mov_b32 s29, 8192\n"
    "v_mfma_f32_16x16x32_bf16 v[200:203], a[68:71], a[212:215], v[200:203]\n"
    "s_waitcnt vmcnt(8) lgkmcnt(7)\n"
    "buffer_load_dwordx4 %25, %22, s29 offen offset:0 lds\n"
    "ds_read_b128 a[112:115], %26 offset:12288\n"
    "v_mfma_f32_16x16x32_bf16 v[200:203], a[80:83], a[216:219], v[200:203]\n"
    "ds_read_b128 a[120:123], %26 offset:13312\n"
    "ds_read_b128 a[192:195], %8 offset:40960\n"
    "v_mfma_f32_16x16x32_bf16 v[200:203], a[84:87], a[220:223], v[200:203]\n"
    "ds_read_b128 a[196:199], %9 offset:40960\n"
    "ds_read_b128 a[200:203], %8 offset:57344\n"
    "v_mfma_f32_16x16x4_4b_bf16 v[0:15], a[240:241], a[248:249], v[0:15]\n"
    "ds_read_b128 a[204:207], %9 offset:57344\n"
    "v_mfma_f32_16x16x32_bf16 v[216:219], a[72:75], a[208:211], v[216:219]\n"
    "buffer_load_dwordx4 %25, %22, s29 offen offset:1024 lds\n"
    "v_mfma_f32_16x16x32_bf16 v[216:219], a[76:79], a[212:215], v[216:219]\n"
    "v_mfma_f32_16x16x32_bf16 v[216:219], a[88:91], a[216:219], v[216:219]\n"
    "v_mfma_f32_16x16x32_bf16 v[216:219], a[92:95], a[220:223], v[216:219]\n"
    "ds_read_u16 v242, %0 offset:6336\n"
    "s_waitcnt lgkmcnt(0)\n"
    "v_cmp_eq_u32 vcc, 0, v242\n"
    "s_cbranch_vccnz .Lslow6f_%=\n"
    ".Lchk6f_%=:\n"
    "s_mov_b64 exec, 1\n"
    "buffer_store_dwordx2 a[252:253], off, %17, 0 sc1\n"
    "s_mov_b64 exec, -1\n"
    "s_nop 3\n"
    "v_mfma_f32_16x16x32_bf16 v[204:207], a[64:67], a[224:227], v[204:207]\n"
    "ds_read_b128 a[116:119], %27 offset:12288\n"
    "ds_read_b128 a[124:127], %27 offset:13312\n"
    "v_mfma_f32_16x16x32_bf16 v[204:207], a[68:71], a[228:231], v[204:207]\n"
    "buffer_load_dwordx4 %25, %22, s29 offen offset:2048 lds\n"
    "ds_read_b128 a[208:211], %8 offset:43008\n"
    "v_mfma_f32_16x16x32_bf16 v[204:207], a[80:83], a[232:235], v[204:207]\n"
    "ds_read_b128 a[212:215], %9 offset:43008\n"
    "ds_read_b128 a[216:219], %8 offset:59392\n"
    "v_mfma_f32_16x16x32_bf16 v[204:207], a[84:87], a[236:239], v[204:207]\n"
    "ds_read_b128 a[220:223], %9 offset:59392\n"
    "v_mfma_f32_16x16x4_4b_bf16 v[16:31], a[242:243], a[248:249], v[16:31]\n"
    "v_mfma_f32_16x16x32_bf16 v[220:223], a[72:75], a[224:227], v[220:223]\n"
    "buffer_load_dwordx4 %25, %22, s29 offen offset:3072 lds\n"
    "v_mfma_f32_16x16x32_bf16 v[220:223], a[76:79], a[228:231], v[220:223]\n"
    "v_mfma_f32_16x16x32_bf16 v[220:223], a[88:91], a[232:235], v[220:223]\n"
    "v_mfma_f32_16x16x32_bf16 v[220:223], a[92:95], a[236:239], v[220:223]\n"
    "v_mfma_f32_16x16x32_bf16 v[192:195], a[96:99], a[192:195], v[192:195]\n"
    "s_add_u32 m0, %23, 12288\n"
    "s_mov_b32 s29, 8320\n"
    "v_mfma_f32_16x16x32_bf16 v[192:195], a[100:103], a[196:199], v[192:195]\n"
    "s_waitcnt vmcnt(9) lgkmcnt(4)\n"
    "buffer_load_dwordx4 %25, %22, s29 offen offset:0 lds\n"
    "ds_read_b128 a[0:3], %28 offset:0\n"
    "v_mfma_f32_16x16x32_bf16 v[192:195], a[112:115], a[200:203], v[192:195]\n"
    "ds_read_b128 a[8:11], %28 offset:1024\n"
    "ds_read_b128 a[224:227], %8 offset:45056\n"
    "v_mfma_f32_16x16x32_bf16 v[192:195], a[116:119], a[204:207], v[192:195]\n"
    "ds_read_b128 a[228:231], %9 offset:45056\n"
    "ds_read_b128 a[232:235], %8 offset:61440\n"
    "v_mfma_f32_16x16x32_bf16 v[208:211], a[104:107], a[192:195], v[208:211]\n"
    "ds_read_b128 a[236:239], %9 offset:61440\n"
    "ds_read_u16 a240, %0 offset:128\n"
    "v_mfma_f32_16x16x32_bf16 v[208:211], a[108:111], a[196:199], v[208:211]\n"
    "buffer_load_dwordx4 %25, %22, s29 offen offset:1024 lds\n"
    "ds_read_u16 a242, %0 offset:136\n"
    "v_mfma_f32_16x16x32_bf16 v[208:211], a[120:123], a[200:203], v[208:211]\n"
    "v_mfma_f32_16x16x32_bf16 v[208:211], a[124:127], a[204:207], v[208:211]\n"
    "s_waitcnt lgkmcnt(8)\n"
    "v_mfma_f32_16x16x32_bf16 v[196:199], a[96:99], a[208:211], v[196:199]\n"
    "ds_read_b128 a[4:7], %29 offset:0\n"
    "ds_read_b128 a[12:15], %29 offset:1024\n"
    "v_mfma_f32_16x16x32_bf16 v[196:199], a[100:103], a[212:215], v[196:199]\n"
    "buffer_load_dwordx4 %25, %22, s29 offen offset:2048 lds\n"
    "ds_read_b128 a[192:195], %8 offset:47104\n"
    "v_add_u32 v240, %5, %10\n"
    "v_cvt_pk_bf16_f32 v224, v192, v208\n"
    "v_mfma_f32_16x16x32_bf16 v[196:199], a[112:115], a[216:219], v[196:199]\n"
    "ds_read_b128 a[196:199], %9 offset:47104\n"
    "ds_read_b128 a[200:203], %8 offset:63488\n"
    "v_cvt_pk_bf16_f32 v225, v193, v209\n"
    "v_cvt_pk_bf16_f32 v226, v194, v210\n"
    "v_cvt_pk_bf16_f32 v227, v195, v211\n"
    "v_mfma_f32_16x16x32_bf16 v[196:199], a[116:119], a[220:223], v[196:199]\n"
    "ds_write_b128 v240, v[224:227] offset:0\n"
    "v_mfma_f32_16x16x32_bf16 v[212:215], a[104:107], a[208:211], v[212:215]\n"
    "v_mfma_f32_16x16x32_bf16 v[212:215], a[108:111], a[212:215], v[212:215]\n"
    "ds_write_b16 %1, a248 offset:64\n"
    "v_mfma_f32_16x16x32_bf16 v[212:215], a[120:123], a[216:219], v[212:215]\n"
    "v_mfma_f32_16x16x32_bf16 v[212:215], a[124:127], a[220:223], v[212:215]\n"
    "s_waitcnt lgkmcnt(7)\n"
    "buffer_load_dwordx4 %25, %22, s29 offen offset:3072 lds\n"
    "ds_read_b128 a[204:207], %9 offset:63488\n"
    "v_mfma_f32_16x16x32_bf16 v[200:203], a[96:99], a[224:227], v[200:203]\n"
    "s_add_u32 m0, %23, 0\n"
    "s_mov_b32 s29, 16384\n"
    "v_mfma_f32_16x16x32_bf16 v[200:203], a[100:103], a[228:231], v[200:203]\n"
    "s_waitcnt vmcnt(9) lgkmcnt(6)\n"
    "buffer_load_dwordx4 %25, %22, s29 offen offset:0 lds\n"
    "ds_read_b128 a[16:19], %28 offset:4096\n"
    "v_cvt_pk_bf16_f32 v228, v196, v212\n"
    "v_mfma_f32_16x16x32_bf16 v[200:203], a[112:115], a[232:235], v[200:203]\n"
    "ds_read_b128 a[24:27], %28 offset:5120\n"
    "ds_read_u16 v242, %0 offset:6208\n"
    "v_cvt_pk_bf16_f32 v229, v197, v213\n"
    "v_cvt_pk_bf16_f32 v230, v198, v214\n"
    "v_cvt_pk_bf16_f32 v231, v199, v215\n"
    "v_mfma_f32_16x16x32_bf16 v[200:203], a[116:119], a[236:239], v[200:203]\n"
    "ds_write_b128 v240, v[228:231] offset:1024\n"
    "v_mfma_f32_16x16x32_bf16 v[216:219], a[104:107], a[224:227], v[216:219]\n"
    "v_mfma_f32_16x16x32_bf16 v[216:219], a[108:111], a[228:231], v[216:219]\n"
    "ds_write_b16 %1, a248 offset:72\n"
    "s_waitcnt lgkmcnt(2)\n"
    "v_cmp_eq_u32 vcc, 0, v242\n"
    "v_mfma_f32_16x16x32_bf16 v[216:219], a[120:123], a[232:235], v[216:219]\n"
    "v_mfma_f32_16x16x32_bf16 v[216:219], a[124:127], a[236:239], v[216:219]\n"
    "s_cbranch_vccnz .Lslow2f_%=\n"
    ".Lchk2f_%=:\n"
    "buffer_load_dwordx4 %25, %22, s29 offen offset:1024 lds\n"
    "v_mfma_f32_16x16x32_bf16 v[204:207], a[96:99], a[192:195], v[204:207]\n"
    "ds_read_b128 a[128:131], %10 offset:0\n"
    "ds_read_b128 a[144:147], %10 offset:4096\n"
    "v_mfma_f32_16x16x32_bf16 v[204:207], a[100:103], a[196:199], v[204:207]\n"
    "buffer_load_dwordx4 %25, %22, s29 offen offset:2048 lds\n"
    "ds_read_u16 v242, %0 offset:6216\n"
    "v_cvt_pk_bf16_f32 v232, v200, v216\n"
    "v_mfma_f32_16x16x32_bf16 v[204:207], a[112:115], a[200:203], v[204:207]\n"
    "ds_read_b128 a[160:163], %10 offset:8192\n"
    "ds_read_b128 a[176:179], %10 offset:12288\n"
    "v_cvt_pk_bf16_f32 v233, v201, v217\n"
    "v_cvt_pk_bf16_f32 v234, v202, v218\n"
    "v_cvt_pk_bf16_f32 v235, v203, v219\n"
    "v_mfma_f32_16x16x32_bf16 v[204:207], a[116:119], a[204:207], v[204:207]\n"
    "ds_write_b128 v240, v[232:235] offset:2048\n"
    "v_mfma_f32_16x16x32_bf16 v[220:223], a[104:107], a[192:195], v[220:223]\n"
    "s_waitcnt lgkmcnt(3)\n"
    "v_cmp_eq_u32 vcc, 0, v242\n"
    "v_mfma_f32_16x16x32_bf16 v[220:223], a[108:111], a[196:199], v[220:223]\n"
    "ds_write_b16 %1, a248 offset:128\n"
    "v_lshlrev_b32 v228, 16, v130\n"
    "v_mfma_f32_16x16x32_bf16 v[220:223], a[120:123], a[200:203], v[220:223]\n"
    "s_cbranch_vccnz .Lslow3f_%=\n"
    ".Lchk3f_%=:\n"
    "v_and_b32 v229, s25, v130\n"
    "v_lshlrev_b32 v230, 16, v131\n"
    "v_and_b32 v231, s25, v131\n"
    "v_mfma_f32_16x16x32_bf16 v[220:223], a[124:127], a[204:207], v[220:223]\n"
    "buffer_load_dwordx4 %25, %22, s29 offen offset:3072 lds\n"
    "ds_read_b128 a[20:23], %29 offset:4096\n"
    "ds_read_b128 a[28:31], %29 offset:5120\n"
    "v_mfma_f32_16x16x32_bf16 v[0:3], a[0:3], a[128:131], v[0:3]\n"
    "s_add_u32 m0, %23, 4096\n"
    "ds_read_b128 a[132:135], %10 offset:1024\n"
    "ds_read_b128 a[148:151], %10 offset:5120\n"
    "s_mov_b32 s29, 16512\n"
    "v_mfma_f32_16x16x32_bf16 v[0:3], a[4:7], a[144:147], v[0:3]\n"
    "s_waitcnt lgkmcnt(2)\n"
    "buffer_load_dwordx4 %25, %22, s29 offen offset:0 lds\n"
    "ds_read_u16 v242, %0 offset:6272\n"
    "v_cvt_pk_bf16_f32 v236, v204, v220\n"
    "v_mfma_f32_16x16x32_bf16 v[0:3], a[16:19], a[160:163], v[0:3]\n"
    "ds_read_b128 a[164:167], %10 offset:9216\n"
    "ds_read_b128 a[180:183], %10 offset:13312\n"
    "v_cvt_pk_bf16_f32 v237, v205, v221\n"
    "v_cvt_pk_bf16_f32 v238, v206, v222\n"
    "v_cvt_pk_bf16_f32 v239, v207, v223\n"
    "v_mfma_f32_16x16x32_bf16 v[0:3], a[20:23], a[176:179], v[0:3]\n"
    "ds_write_b128 v240, v[236:239] offset:3072\n"
    "v_mfma_f32_16x16x32_bf16 v[16:19], a[8:11], a[128:131], v[16:19]\n"
    "s_waitcnt lgkmcnt(3)\n"
    "v_cmp_eq_u32 vcc, 0, v242\n"
    "v_mfma_f32_16x16x32_bf16 v[16:19], a[12:15], a[144:147], v[16:19]\n"
    "ds_write_b16 %1, a248 offset:136\n"
    "v_lshlrev_b32 v224, 16, v128\n"
    "v_and_b32 v225, s25, v128\n"
    "v_lshlrev_b32 v226, 16, v129\n"
    "v_mfma_f32_16x16x32_bf16 v[16:19], a[24:27], a[160:163], v[16:19]\n"
    "s_cbranch_vccnz .Lslow4f_%=\n"
    ".Lchk4f_%=:\n"
    "v_and_b32 v227, s25, v129\n"
    "v_lshlrev_b32 v236, 16, v134\n"
    "v_and_b32 v237, s25, v134\n"
    "v_lshlrev_b32 v238, 16, v135\n"
    "v_and_b32 v239, s25, v135\n"
    "v_mfma_f32_16x16x32_bf16 v[16:19], a[28:31], a[176:179], v[16:19]\n"
    "s_waitcnt vmcnt(9)\n"
    "buffer_load_dwordx4 %25, %22, s29 offen offset:1024 lds\n"
    "ds_read_b128 a[32:35], %28 offset:8192\n"
    "ds_read_b128 a[40:43], %28 offset:9216\n"
    "v_mfma_f32_16x16x32_bf16 v[4:7], a[0:3], a[132:135], v[4:7]\n"
    "ds_read_b128 a[136:139], %10 offset:2048\n"
    "ds_read_b128 a[152:155], %10 offset:6144\n"
    "v_lshlrev_b32 v232, 16, v132\n"
    "v_and_b32 v233, s25, v132\n"
    "v_lshlrev_b32 v234, 16, v133\n"
    "v_and_b32 v235, s25, v133\n"
    "v_pk_mul_f32 v[224:225], v[224:225], v[0:1]\n"
    "v_pk_mul_f32 v[226:227], v[226:227], v[2:3]\n"
    "v_pk_mul_f32 v[228:229], v[228:229], v[16:17]\n"
    "v_pk_mul_f32 v[230:231], v[230:231], v[18:19]\n"
    "v_mfma_f32_16x16x32_bf16 v[4:7], a[4:7], a[148:151], v[4:7]\n"
    "s_waitcnt lgkmcnt(4)\n"
    "ds_read_u16 v242, %0 offset:6280\n"
    "ds_read_b128 a[168:171], %10 offset:10240\n"
    "v_add_u32 v240, %4, %8\n"
    "v_cvt_pk_bf16_f32 v224, v224, v225\n"
    "v_mfma_f32_16x16x32_bf16 v[4:7], a[16:19], a[164:167], v[4:7]\n"
    "buffer_load_dwordx4 %25, %22, s29 offen offset:2048 lds\n"
    "ds_read_b128 a[184:187], %10 offset:14336\n"
    "v_cvt_pk_bf16_f32 v225, v226, v227\n"
    "v_mfma_f32_16x16x32_bf16 v[4:7], a[20:23], a[180:183], v[4:7]\n"
    "ds_read_b128 a[36:39], %29 offset:8192\n"
    "ds_read_b128 a[44:47], %29 offset:9216\n"
    "v_cvt_pk_bf16_f32 v226, v228, v229\n"
    "v_cvt_pk_bf16_f32 v227, v230, v231\n"
    "v_mfma_f32_16x16x32_bf16 v[20:23], a[8:11], a[132:135], v[20:23]\n"
    "ds_write_b128 v240, v[224:227] offset:0\n"
    "s_waitcnt lgkmcnt(5)\n"
    "v_cmp_eq_u32 vcc, 0, v242\n"
    "v_mfma_f32_16x16x32_bf16 v[20:23], a[12:15], a[148:151], v[20:23]\n"
    "v_lshlrev_b32 v228, 16, v138\n"
    "v_and_b32 v229, s25, v138\n"
    "v_mfma_f32_16x16x32_bf16 v[20:23], a[24:27], a[164:167], v[20:23]\n"
    "s_cbranch_vccnz .Lslow5f_%=\n"
    ".Lchk5f_%=:\n"
    "v_lshlrev_b32 v230, 16, v139\n"
    "v_and_b32 v231, s25, v139\n"
    "v_mfma_f32_16x16x32_bf16 v[20:23], a[28:31], a[180:183], v[20:23]\n"
    "buffer_load_dwordx4 %25, %22, s29 offen offset:3072 lds\n"
    "v_mfma_f32_16x16x32_bf16 v[8:11], a[0:3], a[136:139], v[8:11]\n"
    "s_mov_b32 s29, 24576\n"
    "ds_read_b128 a[140:143], %10 offset:3072\n"
    "ds_read_b128 a[156:159], %10 offset:7168\n"
    "v_lshlrev_b32 v224, 16, v136\n"
    "v_and_b32 v225, s25, v136\n"
    "v_lshlrev_b32 v226, 16, v137\n"
    "v_and_b32 v227, s25, v137\n"
    "v_pk_mul_f32 v[232:233], v[232:233], v[4:5]\n"
    "v_pk_mul_f32 v[234:235], v[234:235], v[6:7]\n"
    "v_pk_mul_f32 v[236:237], v[236:237], v[20:21]\n"
    "v_pk_mul_f32 v[238:239], v[238:239], v[22:23]\n"
    "v_mfma_f32_16x16x32_bf16 v[8:11], a[4:7], a[152:155], v[8:11]\n"
    "s_add_u32 m0, %23, 8192\n"
    "ds_read_b128 a[172:175], %10 offset:11264\n"
    "ds_read_b128 a[188:191], %10 offset:15360\n"
    "v_cvt_pk_bf16_f32 v232, v232, v233\n"
    "v_cvt_pk_bf16_f32 v233, v234, v235\n"
    "s_waitcnt lgkmcnt(4)\n"
    "v_mfma_f32_16x16x32_bf16 v[8:11], a[16:19], a[168:171], v[8:11]\n"
    "s_waitcnt vmcnt(8)\n"
    "buffer_load_dwordx4 %25, %22, s29 offen offset:0 lds\n"
    "ds_read_b128 a[48:51], %28 offset:12288\n"
    "v_mfma_f32_16x16x32_bf16 v[8:11], a[20:23], a[184:187], v[8:11]\n"
    "ds_read_b128 a[56:59], %28 offset:13312\n"
    "v_cvt_pk_bf16_f32 v234, v236, v237\n"
    "v_cvt_pk_bf16_f32 v235, v238, v239\n"
    "v_mfma_f32_16x16x4_4b_bf16 v[32:47], a[244:245], a[248:249], v[32:47]\n"
    "ds_write_b128 v240, v[232:235] offset:2048\n"
    "v_mfma_f32_16x16x32_bf16 v[24:27], a[8:11], a[136:139], v[24:27]\n"
    "v_lshlrev_b32 v236, 16, v142\n"
    "v_mfma_f32_16x16x32_bf16 v[24:27], a[12:15], a[152:155], v[24:27]\n"
    "v_and_b32 v237, s25, v142\n"
    "v_lshlrev_b32 v238, 16, v143\n"
    "v_mfma_f32_16x16x32_bf16 v[24:27], a[24:27], a[168:171], v[24:27]\n"
    "v_and_b32 v239, s25, v143\n"
    "v_mfma_f32_16x16x32_bf16 v[24:27], a[28:31], a[184:187], v[24:27]\n"
    "s_waitcnt lgkmcnt(3)\n"
    "buffer_load_dwordx4 %25, %22, s29 offen offset:1024 lds\n"
    "v_lshrrev_b32 v196, 1, %10\n"
    "v_add_u32 v196, %20, v196\n"
    "v_accvgpr_read_b32 v194, a252\n"
    "v_accvgpr_read_b32 v195, a253\n"
    "v_mfma_f32_16x16x32_bf16 v[12:15], a[0:3], a[140:143], v[12:15]\n"
    "ds_read_b128 a[52:55], %29 offset:12288\n"
    "ds_read_b128 a[60:63], %29 offset:13312\n"
    "v_lshlrev_b32 v232, 16, v140\n"
    "v_and_b32 v233, s25, v140\n"
    "v_lshlrev_b32 v234, 16, v141\n"
    "v_and_b32 v235, s25, v141\n"
    "v_pk_mul_f32 v[224:225], v[224:225], v[8:9]\n"
    "v_pk_mul_f32 v[226:227], v[226:227], v[10:11]\n"
    "v_pk_mul_f32 v[228:229], v[228:229], v[24:25]\n"
    "v_pk_mul_f32 v[230:231], v[230:231], v[26:27]\n"
    "v_mfma_f32_16x16x32_bf16 v[12:15], a[4:7], a[156:159], v[12:15]\n"
    "buffer_load_dwordx4 %25, %22, s29 offen offset:2048 lds\n"
    "v_cvt_pk_bf16_f32 v224, v224, v225\n"
    "v_mfma_f32_16x16x32_bf16 v[12:15], a[16:19], a[172:175], v[12:15]\n"
    "v_cvt_pk_bf16_f32 v225, v226, v227\n"
    "v_cvt_pk_bf16_f32 v226, v228, v229\n"
    "v_cvt_pk_bf16_f32 v227, v230, v231\n"
    "v_mfma_f32_16x16x32_bf16 v[12:15], a[20:23], a[188:191], v[12:15]\n"
    "buffer_load_dwordx4 %25, %22, s29 offen offset:3072 lds\n"
    "v_mfma_f32_16x16x4_4b_bf16 v[48:63], a[246:247], a[248:249], v[48:63]\n"
    "ds_write_b128 v240, v[224:227] offset:4096\n"
    "v_mfma_f32_16x16x32_bf16 v[28:31], a[8:11], a[140:143], v[28:31]\n"
    "v_lshlrev_b32 v228, 16, v146\n"
    "v_mfma_f32_16x16x32_bf16 v[28:31], a[12:15], a[156:159], v[28:31]\n"
    "v_and_b32 v229, s25, v146\n"
    "v_lshlrev_b32 v230, 16, v147\n"
    "v_mfma_f32_16x16x32_bf16 v[28:31], a[24:27], a[172:175], v[28:31]\n"
    "v_and_b32 v231, s25, v147\n"
    "v_mfma_f32_16x16x32_bf16 v[28:31], a[28:31], a[188:191], v[28:31]\n"
    "s_waitcnt lgkmcnt(1)\n"
    "buffer_load_dwordx2 v[192:193], v196, %15, 0 offen sc1\n"
    "v_mfma_f32_16x16x32_bf16 v[32:35], a[32:35], a[128:131], v[32:35]\n"
    "s_mov_b32 s29, 24704\n"
    "v_lshlrev_b32 v224, 16, v144\n"
    "v_and_b32 v225, s25, v144\n"
    "v_lshlrev_b32 v226, 16, v145\n"
    "v_and_b32 v227, s25, v145\n"
    "v_pk_mul_f32 v[232:233], v[232:233], v[12:13]\n"
    "v_pk_mul_f32 v[234:235], v[234:235], v[14:15]\n"
    "v_pk_mul_f32 v[236:237], v[236:237], v[28:29]\n"
    "v_pk_mul_f32 v[238:239], v[238:239], v[30:31]\n"
    "v_mfma_f32_16x16x32_bf16 v[32:35], a[36:39], a[144:147], v[32:35]\n"
    "s_add_u32 m0, %23, 12288\n"
    "v_cvt_pk_bf16_f32 v232, v232, v233\n"
    "v_cvt_pk_bf16_f32 v233, v234, v235\n"
    "v_cvt_pk_bf16_f32 v234, v236, v237\n"
    "v_mfma_f32_16x16x32_bf16 v[32:35], a[48:51], a[160:163], v[32:35]\n"
    "s_waitcnt vmcnt(9)\n"
    "buffer_load_dwordx4 %25, %22, s29 offen offset:0 lds\n"
    "ds_read_b128 a[64:67], %28 offset:0\n"
    "v_mfma_f32_16x16x32_bf16 v[32:35], a[52:55], a[176:179], v[32:35]\n"
    "ds_read_b128 a[72:75], %28 offset:1024\n"
    "v_cvt_pk_bf16_f32 v235, v238, v239\n"
    "v_mfma_f32_16x16x32_bf16 v[48:51], a[40:43], a[128:131], v[48:51]\n"
    "ds_write_b128 v240, v[232:235] offset:6144\n"
    "v_mfma_f32_16x16x32_bf16 v[48:51], a[44:47], a[144:147], v[48:51]\n"
    "v_lshlrev_b32 v236, 16, v150\n"
    "v_and_b32 v237, s25, v150\n"
    "v_mfma_f32_16x16x32_bf16 v[48:51], a[56:59], a[160:163], v[48:51]\n"
    "v_lshlrev_b32 v238, 16, v151\n"
    "v_and_b32 v239, s25, v151\n"
    "v_mfma_f32_16x16x32_bf16 v[48:51], a[60:63], a[176:179], v[48:51]\n"
    "buffer_load_dwordx4 %25, %22, s29 offen offset:1024 lds\n"
    "v_mfma_f32_16x16x32_bf16 v[36:39], a[32:35], a[132:135], v[36:39]\n"
    "ds_read_b128 a[68:71], %29 offset:0\n"
    "ds_read_b128 a[76:79], %29 offset:1024\n"
    "v_lshlrev_b32 v232, 16, v148\n"
    "v_and_b32 v233, s25, v148\n"
    "v_lshlrev_b32 v234, 16, v149\n"
    "v_and_b32 v235, s25, v149\n"
    "v_pk_mul_f32 v[224:225], v[224:225], v[32:33]\n"
    "v_pk_mul_f32 v[226:227], v[226:227], v[34:35]\n"
    "v_pk_mul_f32 v[228:229], v[228:229], v[48:49]\n"
    "v_pk_mul_f32 v[230:231], v[230:231], v[50:51]\n"
    "v_mfma_f32_16x16x32_bf16 v[36:39], a[36:39], a[148:151], v[36:39]\n"
    "ds_read_u16 a244, %0 offset:192\n"
    "v_add_u32 v241, %4, %9\n"
    "v_cvt_pk_bf16_f32 v224, v224, v225\n"
    "v_mfma_f32_16x16x32_bf16 v[36:39], a[48:51], a[164:167], v[36:39]\n"
    "buffer_load_dwordx4 %25, %22, s29 offen offset:2048 lds\n"
    "v_mfma_f32_16x16x32_bf16 v[36:39], a[52:55], a[180:183], v[36:39]\n"
    "v_cvt_pk_bf16_f32 v225, v226, v227\n"
    "v_cvt_pk_bf16_f32 v226, v228, v229\n"
    "v_cvt_pk_bf16_f32 v227, v230, v231\n"
    "v_mfma_f32_16x16x4_4b_bf16 v[64:79], a[240:241], a[248:249], v[64:79]\n"
    "ds_write_b128 v241, v[224:227] offset:0\n"
    "v_mfma_f32_16x16x32_bf16 v[52:55], a[40:43], a[132:135], v[52:55]\n"
    "v_lshlrev_b32 v228, 16, v154\n"
    "v_mfma_f32_16x16x32_bf16 v[52:55], a[44:47], a[148:151], v[52:55]\n"
    "v_and_b32 v229, s25, v154\n"
    "v_lshlrev_b32 v230, 16, v155\n"
    "v_mfma_f32_16x16x32_bf16 v[52:55], a[56:59], a[164:167], v[52:55]\n"
    "v_and_b32 v231, s25, v155\n"
    "v_mfma_f32_16x16x32_bf16 v[52:55], a[60:63], a[180:183], v[52:55]\n"
    "buffer_load_dwordx4 %25, %22, s29 offen offset:3072 lds\n"
    "v_mfma_f32_16x16x32_bf16 v[40:43], a[32:35], a[136:139], v[40:43]\n"
    "s_waitcnt vmcnt(9)\n"
    "ds_read_b128 a[80:83], %28 offset:4096\n"
    "ds_read_b128 a[88:91], %28 offset:5120\n"
    "v_lshlrev_b32 v224, 16, v152\n"
    "v_and_b32 v225, s25, v152\n"
    "v_lshlrev_b32 v226, 16, v153\n"
    "v_and_b32 v227, s25, v153\n"
    "v_pk_mul_f32 v[232:233], v[232:233], v[36:37]\n"
    "v_pk_mul_f32 v[234:235], v[234:235], v[38:39]\n"
    "v_pk_mul_f32 v[236:237], v[236:237], v[52:53]\n"
    "v_pk_mul_f32 v[238:239], v[238:239], v[54:55]\n"
    "v_mfma_f32_16x16x32_bf16 v[40:43], a[36:39], a[152:155], v[40:43]\n"
    "ds_read_u16 a246, %0 offset:200\n"
    "v_cvt_pk_bf16_f32 v232, v232, v233\n"
    "v_mfma_f32_16x16x32_bf16 v[40:43], a[48:51], a[168:171], v[40:43]\n"
    "v_cvt_pk_bf16_f32 v233, v234, v235\n"
    "v_mfma_f32_16x16x32_bf16 v[40:43], a[52:55], a[184:187], v[40:43]\n"
    "v_cvt_pk_bf16_f32 v234, v236, v237\n"
    "v_cvt_pk_bf16_f32 v235, v238, v239\n"
    "v_mfma_f32_16x16x4_4b_bf16 v[80:95], a[242:243], a[248:249], v[80:95]\n"
    "ds_write_b128 v241, v[232:235] offset:2048\n"
    "v_mfma_f32_16x16x32_bf16 v[56:59], a[40:43], a[136:139], v[56:59]\n"
    "v_lshlrev_b32 v236, 16, v158\n"
    "v_mfma_f32_16x16x32_bf16 v[56:59], a[44:47], a[152:155], v[56:59]\n"
    "v_and_b32 v237, s25, v158\n"
    "v_mfma_f32_16x16x32_bf16 v[56:59], a[56:59], a[168:171], v[56:59]\n"
    "v_lshlrev_b32 v238, 16, v159\n"
    "v_mfma_f32_16x16x32_bf16 v[56:59], a[60:63], a[184:187], v[56:59]\n"
    "v_and_b32 v239, s25, v159\n"
    "s_waitcnt vmcnt(4)\n"
    "v_cmp_ne_u64 vcc, v[192:193], v[194:195]\n"
    "s_mov_b32 s29, 0x80000\n"
    "s_cbranch_vccnz .Lpoll%=\n"
    ".Lpoll_e%=:\n"
    "v_mfma_f32_16x16x32_bf16 v[44:47], a[32:35], a[140:143], v[44:47]\n"
    "s_add_u32 s22, s21, 0\n"
    "ds_read_b128 a[84:87], %29 offset:4096\n"
    "ds_read_b128 a[92:95], %29 offset:5120\n"
    "v_lshlrev_b32 v232, 16, v156\n"
    "v_and_b32 v233, s25, v156\n"
    "v_lshlrev_b32 v234, 16, v157\n"
    "v_and_b32 v235, s25, v157\n"
    "v_pk_mul_f32 v[224:225], v[224:225], v[40:41]\n"
    "v_pk_mul_f32 v[226:227], v[226:227], v[42:43]\n"
    "v_pk_mul_f32 v[228:229], v[228:229], v[56:57]\n"
    "v_pk_mul_f32 v[230:231], v[230:231], v[58:59]\n"
    "v_mfma_f32_16x16x32_bf16 v[44:47], a[36:39], a[156:159], v[44:47]\n"
    "buffer_load_dwordx4 a[0:3], %10, %2, s22 offen offset:0 sc1\n"
    "v_cvt_pk_bf16_f32 v224, v224, v225\n"
    "v_mfma_f32_16x16x32_bf16 v[44:47], a[48:51], a[172:175], v[44:47]\n"
    "v_cvt_pk_bf16_f32 v225, v226, v227\n"
    "v_cvt_pk_bf16_f32 v226, v228, v229\n"
    "v_cvt_pk_bf16_f32 v227, v230, v231\n"
    "v_mfma_f32_16x16x32_bf16 v[44:47], a[52:55], a[188:191], v[44:47]\n"
    "buffer_load_dwordx4 a[4:7], %10, %2, s22 offen offset:1024 sc1\n"
    "v_mfma_f32_16x16x32_bf16 v[60:63], a[40:43], a[140:143], v[60:63]\n"
    "ds_write_b128 v241, v[224:227] offset:4096\n"
    "v_mfma_f32_16x16x32_bf16 v[60:63], a[44:47], a[156:159], v[60:63]\n"
    "v_lshlrev_b32 v228, 16, v162\n"
    "v_and_b32 v229, s25, v162\n"
    "v_mfma_f32_16x16x32_bf16 v[60:63], a[56:59], a[172:175], v[60:63]\n"
    "v_lshlrev_b32 v230, 16, v163\n"
    "v_and_b32 v231, s25, v163\n"
    "v_mfma_f32_16x16x32_bf16 v[60:63], a[60:63], a[188:191], v[60:63]\n"
    "s_waitcnt lgkmcnt(1)\n"
    "buffer_load_dwordx4 a[8:11], %10, %2, s22 offen offset:2048 sc1\n"
    "v_mfma_f32_16x16x32_bf16 v[64:67], a[64:67], a[128:131], v[64:67]\n"
    "ds_read_b128 a[96:99], %28 offset:8192\n"
    "ds_read_b128 a[104:107], %28 offset:9216\n"
    "v_lshlrev_b32 v224, 16, v160\n"
    "v_and_b32 v225, s25, v160\n"
    "v_lshlrev_b32 v226, 16, v161\n"
    "v_and_b32 v227, s25, v161\n"
    "v_pk_mul_f32 v[232:233], v[232:233], v[44:45]\n"
    "v_pk_mul_f32 v[234:235], v[234:235], v[46:47]\n"
    "v_pk_mul_f32 v[236:237], v[236:237], v[60:61]\n"
    "v_pk_mul_f32 v[238:239], v[238:239], v[62:63]\n"
    "v_mfma_f32_16x16x32_bf16 v[64:67], a[68:71], a[144:147], v[64:67]\n"
    "buffer_load_dwordx4 a[12:15], %10, %2, s22 offen offset:3072 sc1\n"
    "v_cvt_pk_bf16_f32 v232, v232, v233\n"
    "v_mfma_f32_16x16x32_bf16 v[64:67], a[80:83], a[160:163], v[64:67]\n"
    "s_add_u32 s23, s21, 4096\n"
    "v_cvt_pk_bf16_f32 v233, v234, v235\n"
    "v_cvt_pk_bf16_f32 v234, v236, v237\n"
    "v_cvt_pk_bf16_f32 v235, v238, v239\n"
    "v_mfma_f32_16x16x32_bf16 v[64:67], a[84:87], a[176:179], v[64:67]\n"
    "buffer_load_dwordx4 a[16:19], %10, %2, s23 offen offset:0 sc1\n"
    "v_mfma_f32_16x16x32_bf16 v[80:83], a[72:75], a[128:131], v[80:83]\n"
    "ds_write_b128 v241, v[232:235] offset:6144\n"
    "v_mfma_f32_16x16x32_bf16 v[80:83], a[76:79], a[144:147], v[80:83]\n"
    "v_lshlrev_b32 v236, 16, v166\n"
    "v_and_b32 v237, s25, v166\n"
    "v_mfma_f32_16x16x32_bf16 v[80:83], a[88:91], a[160:163], v[80:83]\n"
    "v_lshlrev_b32 v238, 16, v167\n"
    "v_and_b32 v239, s25, v167\n"
    "v_mfma_f32_16x16x32_bf16 v[80:83], a[92:95], a[176:179], v[80:83]\n"
    "ds_write_b16 %1, a248 offset:0\n"
    "v_mfma_f32_16x16x32_bf16 v[68:71], a[64:67], a[132:135], v[68:71]\n"
    "v_lshlrev_b32 v232, 16, v164\n"
    "v_and_b32 v233, s25, v164\n"
    "v_lshlrev_b32 v234, 16, v165\n"
    "v_and_b32 v235, s25, v165\n"
    "v_pk_mul_f32 v[224:225], v[224:225], v[64:65]\n"
    "v_pk_mul_f32 v[226:227], v[226:227], v[66:67]\n"
    "v_pk_mul_f32 v[228:229], v[228:229], v[80:81]\n"
    "v_pk_mul_f32 v[230:231], v[230:231], v[82:83]\n"
    "v_mfma_f32_16x16x32_bf16 v[68:71], a[68:71], a[148:151], v[68:71]\n"
    "buffer_load_dwordx4 a[20:23], %10, %2, s23 offen offset:1024 sc1\n"
    "ds_read_b128 a[100:103], %29 offset:8192\n"
    "v_add_u32 v240, %4, %8\n"
    "v_mfma_f32_16x16x32_bf16 v[68:71], a[80:83], a[164:167], v[68:71]\n"
    "ds_read_b128 a[108:111], %29 offset:9216\n"
    "v_cvt_pk_bf16_f32 v224, v224, v225\n"
    "v_cvt_pk_bf16_f32 v225, v226, v227\n"
    "v_cvt_pk_bf16_f32 v226, v228, v229\n"
    "v_mfma_f32_16x16x32_bf16 v[68:71], a[84:87], a[180:183], v[68:71]\n"
    "buffer_load_dwordx4 a[24:27], %10, %2, s23 offen offset:2048 sc1\n"
    "v_cvt_pk_bf16_f32 v227, v230, v231\n"
    "v_mfma_f32_16x16x4_4b_bf16 v[96:111], a[244:245], a[248:249], v[96:111]\n"
    "ds_write_b128 v240, v[224:227] offset:8192\n"
    "v_mfma_f32_16x16x32_bf16 v[84:87], a[72:75], a[132:135], v[84:87]\n"
    "v_lshlrev_b32 v228, 16, v170\n"
    "v_mfma_f32_16x16x32_bf16 v[84:87], a[76:79], a[148:151], v[84:87]\n"
    "v_and_b32 v229, s25, v170\n"
    "v_lshlrev_b32 v230, 16, v171\n"
    "v_mfma_f32_16x16x32_bf16 v[84:87], a[88:91], a[164:167], v[84:87]\n"
    "v_and_b32 v231, s25, v171\n"
    "v_mfma_f32_16x16x32_bf16 v[84:87], a[92:95], a[180:183], v[84:87]\n"
    "buffer_load_dwordx4 a[28:31], %10, %2, s23 offen offset:3072 sc1\n"
    "v_mfma_f32_16x16x32_bf16 v[72:75], a[64:67], a[136:139], v[72:75]\n"
    "s_add_u32 s22, s21, 8192\n"
    "v_lshlrev_b32 v224, 16, v168\n"
    "v_and_b32 v225, s25, v168\n"
    "v_lshlrev_b32 v226, 16, v169\n"
    "v_and_b32 v227, s25, v169\n"
    "v_pk_mul_f32 v[232:233], v[232:233], v[68:69]\n"
    "v_pk_mul_f32 v[234:235], v[234:235], v[70:71]\n"
    "v_pk_mul_f32 v[236:237], v[236:237], v[84:85]\n"
    "v_pk_mul_f32 v[238:239], v[238:239], v[86:87]\n"
    "v_mfma_f32_16x16x32_bf16 v[72:75], a[68:71], a[152:155], v[72:75]\n"
    "s_waitcnt vmcnt(8)\n"
    "buffer_load_dwordx4 a[32:35], %10, %2, s22 offen offset:0 sc1\n"
    "ds_read_b128 a[112:115], %28 offset:12288\n"
    "v_mfma_f32_16x16x32_bf16 v[72:75], a[80:83], a[168:171], v[72:75]\n"
    "ds_read_b128 a[120:123], %28 offset:13312\n"
    "v_cvt_pk_bf16_f32 v232, v232, v233\n"
    "v_cvt_pk_bf16_f32 v233, v234, v235\n"
    "v_cvt_pk_bf16_f32 v234, v236, v237\n"
    "v_mfma_f32_16x16x32_bf16 v[72:75], a[84:87], a[184:187], v[72:75]\n"
    "buffer_load_dwordx4 a[36:39], %10, %2, s22 offen offset:1024 sc1\n"
    "v_cvt_pk_bf16_f32 v235, v238, v239\n"
    "v_mfma_f32_16x16x4_4b_bf16 v[112:127], a[246:247], a[248:249], v[112:127]\n"
    "ds_write_b128 v240, v[232:235] offset:10240\n"
    "v_mfma_f32_16x16x32_bf16 v[88:91], a[72:75], a[136:139], v[88:91]\n"
    "v_lshlrev_b32 v236, 16, v174\n"
    "v_mfma_f32_16x16x32_bf16 v[88:91], a[76:79], a[152:155], v[88:91]\n"
    "v_and_b32 v237, s25, v174\n"
    "v_lshlrev_b32 v238, 16, v175\n"
    "v_mfma_f32_16x16x32_bf16 v[88:91], a[88:91], a[168:171], v[88:91]\n"
    "v_and_b32 v239, s25, v175\n"
    "v_mfma_f32_16x16x32_bf16 v[88:91], a[92:95], a[184:187], v[88:91]\n"
    "buffer_load_dwordx4 a[40:43], %10, %2, s22 offen offset:2048 sc1\n"
    "v_mfma_f32_16x16x32_bf16 v[76:79], a[64:67], a[140:143], v[76:79]\n"
    "ds_read_b128 a[116:119], %29 offset:12288\n"
    "ds_read_b128 a[124:127], %29 offset:13312\n"
    "v_lshlrev_b32 v232, 16, v172\n"
    "v_and_b32 v233, s25, v172\n"
    "v_lshlrev_b32 v234, 16, v173\n"
    "v_and_b32 v235, s25, v173\n"
    "v_pk_mul_f32 v[224:225], v[224:225], v[72:73]\n"
    "v_pk_mul_f32 v[226:227], v[226:227], v[74:75]\n"
    "v_pk_mul_f32 v[228:229], v[228:229], v[88:89]\n"
    "v_pk_mul_f32 v[230:231], v[230:231], v[90:91]\n"
    "v_mfma_f32_16x16x32_bf16 v[76:79], a[68:71], a[156:159], v[76:79]\n"
    "v_cvt_pk_bf16_f32 v224, v224, v225\n"
    "v_cvt_pk_bf16_f32 v225, v226, v227\n"
    "v_mfma_f32_16x16x32_bf16 v[76:79], a[80:83], a[172:175], v[76:79]\n"
    "buffer_load_dwordx4 a[44:47], %10, %2, s22 offen offset:3072 sc1\n"
    "v_mfma_f32_16x16x32_bf16 v[76:79], a[84:87], a[188:191], v[76:79]\n"
    "v_cvt_pk_bf16_f32 v226, v228, v229\n"
    "v_cvt_pk_bf16_f32 v227, v230, v231\n"
    "v_mfma_f32_16x16x32_bf16 v[92:95], a[72:75], a[140:143], v[92:95]\n"
    "ds_write_b128 v240, v[224:227] offset:12288\n"
    "v_mfma_f32_16x16x32_bf16 v[92:95], a[76:79], a[156:159], v[92:95]\n"
    "v_lshlrev_b32 v228, 16, v178\n"
    "v_and_b32 v229, s25, v178\n"
    "v_mfma_f32_16x16x32_bf16 v[92:95], a[88:91], a[172:175], v[92:95]\n"
    "s_add_u32 s23, s21, 12288\n"
    "v_lshlrev_b32 v230, 16, v179\n"
    "v_and_b32 v231, s25, v179\n"
    "v_mfma_f32_16x16x32_bf16 v[92:95], a[92:95], a[188:191], v[92:95]\n"
    "s_waitcnt lgkmcnt(1)\n"
    "buffer_load_dwordx4 a[48:51], %10, %2, s23 offen offset:0 sc1\n"
    "v_mfma_f32_16x16x32_bf16 v[96:99], a[96:99], a[128:131], v[96:99]\n"
    "v_lshlrev_b32 v224, 16, v176\n"
    "v_and_b32 v225, s25, v176\n"
    "v_lshlrev_b32 v226, 16, v177\n"
    "v_and_b32 v227, s25, v177\n"
    "v_pk_mul_f32 v[232:233], v[232:233], v[76:77]\n"
    "v_pk_mul_f32 v[234:235], v[234:235], v[78:79]\n"
    "v_pk_mul_f32 v[236:237], v[236:237], v[92:93]\n"
    "v_pk_mul_f32 v[238:239], v[238:239], v[94:95]\n"
    "v_mfma_f32_16x16x32_bf16 v[96:99], a[100:103], a[144:147], v[96:99]\n"
    "buffer_load_dwordx4 a[52:55], %10, %2, s23 offen offset:1024 sc1\n"
    "v_cvt_pk_bf16_f32 v232, v232, v233\n"
    "v_mfma_f32_16x16x32_bf16 v[96:99], a[112:115], a[160:163], v[96:99]\n"
    "v_cvt_pk_bf16_f32 v233, v234, v235\n"
    "v_cvt_pk_bf16_f32 v234, v236, v237\n"
    "v_cvt_pk_bf16_f32 v235, v238, v239\n"
    "v_mfma_f32_16x16x32_bf16 v[96:99], a[116:119], a[176:179], v[96:99]\n"
    "buffer_load_dwordx4 a[56:59], %10, %2, s23 offen offset:2048 sc1\n"
    "v_mfma_f32_16x16x32_bf16 v[112:115], a[104:107], a[128:131], v[112:115]\n"
    "ds_write_b128 v240, v[232:235] offset:14336\n"
    "v_mfma_f32_16x16x32_bf16 v[112:115], a[108:111], a[144:147], v[112:115]\n"
    "v_lshlrev_b32 v236, 16, v182\n"
    "v_and_b32 v237, s25, v182\n"
    "v_mfma_f32_16x16x32_bf16 v[112:115], a[120:123], a[160:163], v[112:115]\n"
    "v_lshlrev_b32 v238, 16, v183\n"
    "v_and_b32 v239, s25, v183\n"
    "v_mfma_f32_16x16x32_bf16 v[112:115], a[124:127], a[176:179], v[112:115]\n"
    "buffer_load_dwordx4 a[60:63], %10, %2, s23 offen offset:3072 sc1\n"
    "v_mfma_f32_16x16x32_bf16 v[100:103], a[96:99], a[132:135], v[100:103]\n"
    "s_add_u32 s22, s21, 16384\n"
    "ds_read_u16 v242, %0 offset:6144\n"
    "v_lshlrev_b32 v232, 16, v180\n"
    "v_and_b32 v233, s25, v180\n"
    "v_lshlrev_b32 v234, 16, v181\n"
    "v_and_b32 v235, s25, v181\n"
    "v_pk_mul_f32 v[224:225], v[224:225], v[96:97]\n"
    "v_pk_mul_f32 v[226:227], v[226:227], v[98:99]\n"
    "v_pk_mul_f32 v[228:229], v[228:229], v[112:113]\n"
    "v_pk_mul_f32 v[230:231], v[230:231], v[114:115]\n"
    "v_mfma_f32_16x16x32_bf16 v[100:103], a[100:103], a[148:151], v[100:103]\n"
    "buffer_load_dwordx4 a[64:67], %10, %2, s22 offen offset:0 sc1\n"
    "v_add_u32 v241, %4, %9\n"
    "v_mfma_f32_16x16x32_bf16 v[100:103], a[112:115], a[164:167], v[100:103]\n"
    "v_cvt_pk_bf16_f32 v224, v224, v225\n"
    "v_cvt_pk_bf16_f32 v225, v226, v227\n"
    "v_cvt_pk_bf16_f32 v226, v228, v229\n"
    "v_cvt_pk_bf16_f32 v227, v230, v231\n"
    "v_mfma_f32_16x16x32_bf16 v[100:103], a[116:119], a[180:183], v[100:103]\n"
    "s_waitcnt lgkmcnt(0)\n"
    "buffer_load_dwordx4 a[68:71], %10, %2, s22 offen offset:1024 sc1\n"
    "v_cmp_eq_u32 vcc, 0, v242\n"
    "v_mfma_f32_16x16x32_bf16 v[116:119], a[104:107], a[132:135], v[116:119]\n"
    "ds_write_b128 v241, v[224:227] offset:8192\n"
    "v_mfma_f32_16x16x32_bf16 v[116:119], a[108:111], a[148:151], v[116:119]\n"
    "s_cbranch_vccnz .Lslow0f_%=\n"
    ".Lchk0f_%=:\n"
    "v_lshlrev_b32 v228, 16, v186\n"
    "v_and_b32 v229, s25, v186\n"
    "v_mfma_f32_16x16x32_bf16 v[116:119], a[120:123], a[164:167], v[116:119]\n"
    "v_lshlrev_b32 v230, 16, v187\n"
    "v_and_b32 v231, s25, v187\n"
    "v_mfma_f32_16x16x32_bf16 v[116:119], a[124:127], a[180:183], v[116:119]\n"
    "buffer_load_dwordx4 a[72:75], %10, %2, s22 offen offset:2048 sc1\n"
    "v_mfma_f32_16x16x32_bf16 v[104:107], a[96:99], a[136:139], v[104:107]\n"
    "ds_read_b128 a[192:195], %8 offset:0\n"
    "ds_read_b128 a[196:199], %9 offset:0\n"
    "v_lshlrev_b32 v224, 16, v184\n"
    "v_and_b32 v225, s25, v184\n"
    "v_lshlrev_b32 v226, 16, v185\n"
    "v_and_b32 v227, s25, v185\n"
    "v_pk_mul_f32 v[232:233], v[232:233], v[100:101]\n"
    "v_pk_mul_f32 v[234:235], v[234:235], v[102:103]\n"
    "v_pk_mul_f32 v[236:237], v[236:237], v[116:117]\n"
    "v_pk_mul_f32 v[238:239], v[238:239], v[118:119]\n"
    "v_mfma_f32_16x16x32_bf16 v[104:107], a[100:103], a[152:155], v[104:107]\n"
    "ds_read_b128 a[200:203], %8 offset:16384\n"
    "ds_read_b128 a[204:207], %9 offset:16384\n"
    "v_cvt_pk_bf16_f32 v232, v232, v233\n"
    "v_mfma_f32_16x16x32_bf16 v[104:107], a[112:115], a[168:171], v[104:107]\n"
    "buffer_load_dwordx4 a[76:79], %10, %2, s22 offen offset:3072 sc1\n"
    "v_mfma_f32_16x16x32_bf16 v[104:107], a[116:119], a[184:187], v[104:107]\n"
    "v_cvt_pk_bf16_f32 v233, v234, v235\n"
    "v_cvt_pk_bf16_f32 v234, v236, v237\n"
    "v_cvt_pk_bf16_f32 v235, v238, v239\n"
    "v_mfma_f32_16x16x32_bf16 v[120:123], a[104:107], a[136:139], v[120:123]\n"
    "ds_write_b128 v241, v[232:235] offset:10240\n"
    "v_mfma_f32_16x16x32_bf16 v[120:123], a[108:111], a[152:155], v[120:123]\n"
    "v_lshlrev_b32 v236, 16, v190\n"
    "v_and_b32 v237, s25, v190\n"
    "v_mfma_f32_16x16x32_bf16 v[120:123], a[120:123], a[168:171], v[120:123]\n"
    "s_add_u32 s23, s21, 20480\n"
    "v_lshlrev_b32 v238, 16, v191\n"
    "v_and_b32 v239, s25, v191\n"
    "v_mfma_f32_16x16x32_bf16 v[120:123], a[124:127], a[184:187], v[120:123]\n"
    "buffer_load_dwordx4 a[80:83], %10, %2, s23 offen offset:0 sc1\n"
    "v_mfma_f32_16x16x32_bf16 v[108:111], a[96:99], a[140:143], v[108:111]\n"
    "ds_read_b128 a[208:211], %8 offset:2048\n"
    "ds_read_b128 a[212:215], %9 offset:2048\n"
    "v_lshlrev_b32 v232, 16, v188\n"
    "v_and_b32 v233, s25, v188\n"
    "v_lshlrev_b32 v234, 16, v189\n"
    "v_and_b32 v235, s25, v189\n"
    "v_pk_mul_f32 v[224:225], v[224:225], v[104:105]\n"
    "v_pk_mul_f32 v[226:227], v[226:227], v[106:107]\n"
    "v_pk_mul_f32 v[228:229], v[228:229], v[120:121]\n"
    "v_pk_mul_f32 v[230:231], v[230:231], v[122:123]\n"
    "v_mfma_f32_16x16x32_bf16 v[108:111], a[100:103], a[156:159], v[108:111]\n"
    "buffer_load_dwordx4 a[84:87], %10, %2, s23 offen offset:1024 sc1\n"
    "ds_read_b128 a[216:219], %8 offset:18432\n"
    "v_mfma_f32_16x16x32_bf16 v[108:111], a[112:115], a[172:175], v[108:111]\n"
    "ds_read_b128 a[220:223], %9 offset:18432\n"
    "v_cvt_pk_bf16_f32 v224, v224, v225\n"
    "v_cvt_pk_bf16_f32 v225, v226, v227\n"
    "v_cvt_pk_bf16_f32 v226, v228, v229\n"
    "v_mfma_f32_16x16x32_bf16 v[108:111], a[116:119], a[188:191], v[108:111]\n"
    "buffer_load_dwordx4 a[88:91], %10, %2, s23 offen offset:2048 sc1\n"
    "v_cvt_pk_bf16_f32 v227, v230, v231\n"
    "v_mfma_f32_16x16x32_bf16 v[124:127], a[104:107], a[140:143], v[124:127]\n"
    "ds_write_b128 v241, v[224:227] offset:12288\n"
    "v_mfma_f32_16x16x32_bf16 v[124:127], a[108:111], a[156:159], v[124:127]\n"
    "v_mfma_f32_16x16x32_bf16 v[124:127], a[120:123], a[172:175], v[124:127]\n"
    "v_mfma_f32_16x16x32_bf16 v[124:127], a[124:127], a[188:191], v[124:127]\n"
    "buffer_load_dwordx4 a[92:95], %10, %2, s23 offen offset:3072 sc1\n"
    "s_waitcnt vmcnt(16)\n"
    "v_add_u32 %0, 256, %0\n"
    "v_add_u32 %1, 256, %1\n"
    "s_mov_b32 s20, 262144\n"
    "s_mov_b32 s21, 524288\n"
    "s_waitcnt lgkmcnt(0)\n"
    "s_nop 7\n"
    ".Ldcn_loop%=:\n"
    "s_waitcnt lgkmcnt(3)\n"
    "v_mfma_f32_16x16x32_bf16 v[192:195], a[0:3], a[192:195], 0\n"
    "s_add_u32 s22, s20, 24576\n"
    "ds_read_b128 a[224:227], %8 offset:4096\n"
    "ds_read_b128 a[228:231], %9 offset:4096\n"
    "v_pk_mul_f32 v[232:233], v[232:233], v[108:109]\n"
    "v_pk_mul_f32 v[234:235], v[234:235], v[110:111]\n"
    "v_pk_mul_f32 v[236:237], v[236:237], v[124:125]\n"
    "v_pk_mul_f32 v[238:239], v[238:239], v[126:127]\n"
    "v_mfma_f32_16x16x32_bf16 v[192:195], a[4:7], a[196:199], v[192:195]\n"
    "buffer_load_dwordx4 a[96:99], %10, %2, s22 offen offset:0 sc1\n"
    "ds_read_b128 a[232:235], %8 offset:20480\n"
    "v_mfma_f32_16x16x32_bf16 v[192:195], a[16:19], a[200:203], v[192:195]\n"
    "ds_read_b128 a[236:239], %9 offset:20480\n"
    "v_cvt_pk_bf16_f32 v232, v232, v233\n"
    "v_cvt_pk_bf16_f32 v233, v234, v235\n"
    "v_cvt_pk_bf16_f32 v234, v236, v237\n"
    "v_mfma_f32_16x16x32_bf16 v[192:195], a[20:23], a[204:207], v[192:195]\n"
    "buffer_load_dwordx4 a[100:103], %10, %2, s22 offen offset:1024 sc1\n"
    "v_cvt_pk_bf16_f32 v235, v238, v239\n"
    "v_mfma_f32_16x16x32_bf16 v[208:211], a[8:11], a[192:195], 0\n"
    "ds_write_b128 v241, v[232:235] offset:14336\n"
    "v_mfma_f32_16x16x32_bf16 v[208:211], a[12:15], a[196:199], v[208:211]\n"
    "v_mfma_f32_16x16x32_bf16 v[208:211], a[24:27], a[200:203], v[208:211]\n"
    "v_mfma_f32_16x16x32_bf16 v[208:211], a[28:31], a[204:207], v[208:211]\n"
    "ds_write_b16 %1, a248 offset:8\n"
    "v_mfma_f32_16x16x32_bf16 v[196:199], a[0:3], a[208:211], 0\n"
    "v_mfma_f32_16x16x32_bf16 v[196:199], a[4:7], a[212:215], v[196:199]\n"
    "s_waitcnt lgkmcnt(4)\n"
    "buffer_load_dwordx4 a[104:107], %10, %2, s22 offen offset:2048 sc1\n"
    "ds_read_b128 a[192:195], %8 offset:6144\n"
    "v_mfma_f32_16x16x32_bf16 v[196:199], a[16:19], a[216:219], v[196:199]\n"
    "ds_read_b128 a[196:199], %9 offset:6144\n"
    "ds_read_b128 a[200:203], %8 offset:22528\n"
    "v_mfma_f32_16x16x32_bf16 v[196:199], a[20:23], a[220:223], v[196:199]\n"
    "ds_read_b128 a[204:207], %9 offset:22528\n"
    "v_mfma_f32_16x16x32_bf16 v[212:215], a[8:11], a[208:211], 0\n"
    "v_mfma_f32_16x16x32_bf16 v[212:215], a[12:15], a[212:215], v[212:215]\n"
    "buffer_load_dwordx4 a[108:111], %10, %2, s22 offen offset:3072 sc1\n"
    "v_mfma_f32_16x16x32_bf16 v[212:215], a[24:27], a[216:219], v[212:215]\n"
    "v_mfma_f32_16x16x32_bf16 v[212:215], a[28:31], a[220:223], v[212:215]\n"
    "v_mfma_f32_16x16x32_bf16 v[200:203], a[0:3], a[224:227], 0\n"
    "s_add_u32 s23, s20, 28672\n"
    "ds_read_b128 a[208:211], %8 offset:32768\n"
    "ds_read_b128 a[212:215], %9 offset:32768\n"
    "v_mfma_f32_16x16x32_bf16 v[200:203], a[4:7], a[228:231], v[200:203]\n"
    "s_waitcnt lgkmcnt(7)\n"
    "buffer_load_dwordx4 a[112:115], %10, %2, s23 offen offset:0 sc1\n"
    "ds_read_b128 a[216:219], %8 offset:49152\n"
    "v_mfma_f32_16x16x32_bf16 v[200:203], a[16:19], a[232:235], v[200:203]\n"
    "ds_read_b128 a[220:223], %9 offset:49152\n"
    "v_mfma_f32_16x16x32_bf16 v[200:203], a[20:23], a[236:239], v[200:203]\n"
    "v_mfma_f32_16x16x32_bf16 v[216:219], a[8:11], a[224:227], 0\n"
    "v_mfma_f32_16x16x32_bf16 v[216:219], a[12:15], a[228:231], v[216:219]\n"
    "buffer_load_dwordx4 a[116:119], %10, %2, s23 offen offset:1024 sc1\n"
    "v_mfma_f32_16x16x32_bf16 v[216:219], a[24:27], a[232:235], v[216:219]\n"
    "v_mfma_f32_16x16x32_bf16 v[216:219], a[28:31], a[236:239], v[216:219]\n"
    "s_waitcnt lgkmcnt(7)\n"
    "v_mfma_f32_16x16x32_bf16 v[204:207], a[0:3], a[192:195], 0\n"
    "s_waitcnt lgkmcnt(4)\n"
    "ds_read_b128 a[224:227], %8 offset:34816\n"
    "ds_read_b128 a[228:231], %9 offset:34816\n"
    "v_mfma_f32_16x16x32_bf16 v[204:207], a[4:7], a[196:199], v[204:207]\n"
    "buffer_load_dwordx4 a[120:123], %10, %2, s23 offen offset:2048 sc1\n"
    "ds_read_b128 a[232:235], %8 offset:51200\n"
    "v_mfma_f32_16x16x32_bf16 v[204:207], a[16:19], a[200:203], v[204:207]\n"
    "ds_read_b128 a[236:239], %9 offset:51200\n"
    "v_mfma_f32_16x16x32_bf16 v[204:207], a[20:23], a[204:207], v[204:207]\n"
    "v_mfma_f32_16x16x32_bf16 v[220:223], a[8:11], a[192:195], 0\n"
    "v_mfma_f32_16x16x32_bf16 v[220:223], a[12:15], a[196:199], v[220:223]\n"
    "buffer_load_dwordx4 a[124:127], %10, %2, s23 offen offset:3072 sc1\n"
    "v_mfma_f32_16x16x32_bf16 v[220:223], a[24:27], a[200:203], v[220:223]\n"
    "v_mfma_f32_16x16x32_bf16 v[220:223], a[28:31], a[204:207], v[220:223]\n"
    "s_waitcnt vmcnt(16)\n"
    ".Ldcn_mid%=:\n"
    "s_waitcnt lgkmcnt(5)\n"
    "v_mfma_f32_16x16x32_bf16 v[192:195], a[32:35], a[208:211], v[192:195]\n"
    "s_add_u32 s22, s20, 32768\n"
    "ds_read_b128 a[192:195], %8 offset:36864\n"
    "ds_read_b128 a[196:199], %9 offset:36864\n"
    "v_mfma_f32_16x16x32_bf16 v[192:195], a[36:39], a[212:215], v[192:195]\n"
    "buffer_load_dwordx4 a[0:3], %10, %2, s22 offen offset:0 sc1\n"
    "ds_read_b128 a[200:203], %8 offset:53248\n"
    "v_mfma_f32_16x16x32_bf16 v[192:195], a[48:51], a[216:219], v[192:195]\n"
    "s_waitcnt lgkmcnt(7)\n"
    "ds_read_b128 a[204:207], %9 offset:53248\n"
    "v_mfma_f32_16x16x32_bf16 v[192:195], a[52:55], a[220:223], v[192:195]\n"
    "v_mfma_f32_16x16x32_bf16 v[208:211], a[40:43], a[208:211], v[208:211]\n"
    "v_mfma_f32_16x16x32_bf16 v[208:211], a[44:47], a[212:215], v[208:211]\n"
    "buffer_load_dwordx4 a[4:7], %10, %2, s22 offen offset:1024 sc1\n"
    "v_mfma_f32_16x16x32_bf16 v[208:211], a[56:59], a[216:219], v[208:211]\n"
    "v_mfma_f32_16x16x32_bf16 v[208:211], a[60:63], a[220:223], v[208:211]\n"
    "s_waitcnt lgkmcnt(5)\n"
    "v_mfma_f32_16x16x32_bf16 v[196:199], a[32:35], a[224:227], v[196:199]\n"
    "ds_read_u16 v242, %0 offset:6152\n"
    "ds_read_b128 a[208:211], %8 offset:38912\n"
    "v_mfma_f32_16x16x32_bf16 v[196:199], a[36:39], a[228:231], v[196:199]\n"
    "buffer_load_dwordx4 a[8:11], %10, %2, s22 offen offset:2048 sc1\n"
    "ds_read_b128 a[212:215], %9 offset:38912\n"
    "v_mfma_f32_16x16x32_bf16 v[196:199], a[48:51], a[232:235], v[196:199]\n"
    "s_waitcnt lgkmcnt(7)\n"
    "ds_read_b128 a[216:219], %8 offset:55296\n"
    "ds_read_b128 a[220:223], %9 offset:55296\n"
    "v_mfma_f32_16x16x32_bf16 v[196:199], a[52:55], a[236:239], v[196:199]\n"
    "s_waitcnt lgkmcnt(4)\n"
    "v_cmp_eq_u32 vcc, 0, v242\n"
    "v_mfma_f32_16x16x32_bf16 v[212:215], a[40:43], a[224:227], v[212:215]\n"
    "v_mfma_f32_16x16x32_bf16 v[212:215], a[44:47], a[228:231], v[212:215]\n"
    "s_cbranch_vccnz .Lslow1_%=\n"
    ".Lchk1_%=:\n"
    "buffer_load_dwordx4 a[12:15], %10, %2, s22 offen offset:3072 sc1\n"
    "v_mfma_f32_16x16x32_bf16 v[212:215], a[56:59], a[232:235], v[212:215]\n"
    "v_mfma_f32_16x16x32_bf16 v[212:215], a[60:63], a[236:239], v[212:215]\n"
    "v_mfma_f32_16x16x32_bf16 v[200:203], a[32:35], a[192:195], v[200:203]\n"
    "s_add_u32 s23, s20, 36864\n"
    "ds_read_b128 a[224:227], %8 offset:8192\n"
    "ds_read_b128 a[228:231], %9 offset:8192\n"
    "v_mfma_f32_16x16x32_bf16 v[200:203], a[36:39], a[196:199], v[200:203]\n"
    "buffer_load_dwordx4 a[16:19], %10, %2, s23 offen offset:0 sc1\n"
    "ds_read_b128 a[232:235], %8 offset:24576\n"
    "v_mfma_f32_16x16x32_bf16 v[200:203], a[48:51], a[200:203], v[200:203]\n"
    "ds_read_b128 a[236:239], %9 offset:24576\n"
    "v_mfma_f32_16x16x32_bf16 v[200:203], a[52:55], a[204:207], v[200:203]\n"
    "v_mfma_f32_16x16x32_bf16 v[216:219], a[40:43], a[192:195], v[216:219]\n"
    "v_mfma_f32_16x16x32_bf16 v[216:219], a[44:47], a[196:199], v[216:219]\n"
    "buffer_load_dwordx4 a[20:23], %10, %2, s23 offen offset:1024 sc1\n"
    "v_mfma_f32_16x16x32_bf16 v[216:219], a[56:59], a[200:203], v[216:219]\n"
    "v_mfma_f32_16x16x32_bf16 v[216:219], a[60:63], a[204:207], v[216:219]\n"
    "s_waitcnt lgkmcnt(4)\n"
    "v_mfma_f32_16x16x32_bf16 v[204:207], a[32:35], a[208:211], v[204:207]\n"
    "ds_read_b128 a[192:195], %8 offset:10240\n"
    "ds_read_b128 a[196:199], %9 offset:10240\n"
    "v_mfma_f32_16x16x32_bf16 v[204:207], a[36:39], a[212:215], v[204:207]\n"
    "buffer_load_dwordx4 a[24:27], %10, %2, s23 offen offset:2048 sc1\n"
    "ds_read_b128 a[200:203], %8 offset:26624\n"
    "v_mfma_f32_16x16x32_bf16 v[204:207], a[48:51], a[216:219], v[204:207]\n"
    "ds_read_b128 a[204:207], %9 offset:26624\n"
    "v_mfma_f32_16x16x32_bf16 v[204:207], a[52:55], a[220:223], v[204:207]\n"
    "v_mfma_f32_16x16x32_bf16 v[220:223], a[40:43], a[208:211], v[220:223]\n"
    "v_mfma_f32_16x16x32_bf16 v[220:223], a[44:47], a[212:215], v[220:223]\n"
    "buffer_load_dwordx4 a[28:31], %10, %2, s23 offen offset:3072 sc1\n"
    "v_mfma_f32_16x16x32_bf16 v[220:223], a[56:59], a[216:219], v[220:223]\n"
    "v_mfma_f32_16x16x32_bf16 v[220:223], a[60:63], a[220:223], v[220:223]\n"
    "s_waitcnt vmcnt(16) lgkmcnt(6)\n"
    "v_mfma_f32_16x16x32_bf16 v[192:195], a[64:67], a[224:227], v[192:195]\n"
    "s_add_u32 s22, s20, 40960\n"
    "ds_read_b128 a[208:211], %8 offset:12288\n"
    "ds_read_b128 a[212:215], %9 offset:12288\n"
    "v_mfma_f32_16x16x32_bf16 v[192:195], a[68:71], a[228:231], v[192:195]\n"
    "s_waitcnt lgkmcnt(6)\n"
    "buffer_load_dwordx4 a[32:35], %10, %2, s22 offen offset:0 sc1\n"
    "ds_read_b128 a[216:219], %8 offset:28672\n"
    "v_mfma_f32_16x16x32_bf16 v[192:195], a[80:83], a[232:235], v[192:195]\n"
    "ds_read_b128 a[220:223], %9 offset:28672\n"
    "ds_read_u16 a240, %0 offset:0\n"
    "v_mfma_f32_16x16x32_bf16 v[192:195], a[84:87], a[236:239], v[192:195]\n"
    "ds_read_u16 a242, %0 offset:8\n"
    "v_mfma_f32_16x16x32_bf16 v[208:211], a[72:75], a[224:227], v[208:211]\n"
    "v_mfma_f32_16x16x32_bf16 v[208:211], a[76:79], a[228:231], v[208:211]\n"
    "buffer_load_dwordx4 a[36:39], %10, %2, s22 offen offset:1024 sc1\n"
    "v_mfma_f32_16x16x32_bf16 v[208:211], a[88:91], a[232:235], v[208:211]\n"
    "v_mfma_f32_16x16x32_bf16 v[208:211], a[92:95], a[236:239], v[208:211]\n"
    "s_waitcnt lgkmcnt(6)\n"
    "v_mfma_f32_16x16x32_bf16 v[196:199], a[64:67], a[192:195], v[196:199]\n"
    "ds_read_b128 a[224:227], %8 offset:14336\n"
    "ds_read_b128 a[228:231], %9 offset:14336\n"
    "v_mfma_f32_16x16x32_bf16 v[196:199], a[68:71], a[196:199], v[196:199]\n"
    "buffer_load_dwordx4 a[40:43], %10, %2, s22 offen offset:2048 sc1\n"
    "ds_read_b128 a[232:235], %8 offset:30720\n"
    "v_mfma_f32_16x16x32_bf16 v[196:199], a[80:83], a[200:203], v[196:199]\n"
    "ds_read_b128 a[236:239], %9 offset:30720\n"
    "ds_read_u16 a244, %0 offset:64\n"
    "v_mfma_f32_16x16x32_bf16 v[196:199], a[84:87], a[204:207], v[196:199]\n"
    "ds_read_u16 a246, %0 offset:72\n"
    "v_mfma_f32_16x16x32_bf16 v[212:215], a[72:75], a[192:195], v[212:215]\n"
    "v_mfma_f32_16x16x32_bf16 v[212:215], a[76:79], a[196:199], v[212:215]\n"
    "buffer_load_dwordx4 a[44:47], %10, %2, s22 offen offset:3072 sc1\n"
    "v_mfma_f32_16x16x32_bf16 v[212:215], a[88:91], a[200:203], v[212:215]\n"
    "v_mfma_f32_16x16x32_bf16 v[212:215], a[92:95], a[204:207], v[212:215]\n"
    "s_waitcnt lgkmcnt(8)\n"
    "v_mfma_f32_16x16x32_bf16 v[200:203], a[64:67], a[208:211], v[200:203]\n"
    "s_add_u32 s23, s20, 45056\n"
    "ds_read_b128 a[192:195], %8 offset:40960\n"
    "ds_read_b128 a[196:199], %9 offset:40960\n"
    "v_mfma_f32_16x16x32_bf16 v[200:203], a[68:71], a[212:215], v[200:203]\n"
    "buffer_load_dwordx4 a[48:51], %10, %2, s23 offen offset:0 sc1\n"
    "ds_read_b128 a[200:203], %8 offset:57344\n"
    "v_mfma_f32_16x16x32_bf16 v[200:203], a[80:83], a[216:219], v[200:203]\n"
    "ds_read_b128 a[204:207], %9 offset:57344\n"
    "v_mfma_f32_16x16x32_bf16 v[200:203], a[84:87], a[220:223], v[200:203]\n"
    "s_waitcnt lgkmcnt(9)\n"
    "v_mfma_f32_16x16x4_4b_bf16 v[0:15], a[240:241], a[248:249], v[0:15]\n"
    "v_mfma_f32_16x16x32_bf16 v[216:219], a[72:75], a[208:211], v[216:219]\n"
    "buffer_load_dwordx4 a[52:55], %10, %2, s23 offen offset:1024 sc1\n"
    "v_mfma_f32_16x16x32_bf16 v[216:219], a[76:79], a[212:215], v[216:219]\n"
    "v_mfma_f32_16x16x32_bf16 v[216:219], a[88:91], a[216:219], v[216:219]\n"
    "v_mfma_f32_16x16x32_bf16 v[216:219], a[92:95], a[220:223], v[216:219]\n"
    "v_mfma_f32_16x16x32_bf16 v[204:207], a[64:67], a[224:227], v[204:207]\n"
    "s_waitcnt lgkmcnt(4)\n"
    "ds_read_b128 a[208:211], %8 offset:43008\n"
    "ds_read_b128 a[212:215], %9 offset:43008\n"
    "v_mfma_f32_16x16x32_bf16 v[204:207], a[68:71], a[228:231], v[204:207]\n"
    "buffer_load_dwordx4 a[56:59], %10, %2, s23 offen offset:2048 sc1\n"
    "ds_read_b128 a[216:219], %8 offset:59392\n"
    "v_mfma_f32_16x16x32_bf16 v[204:207], a[80:83], a[232:235], v[204:207]\n"
    "ds_read_b128 a[220:223], %9 offset:59392\n"
    "v_mfma_f32_16x16x32_bf16 v[204:207], a[84:87], a[236:239], v[204:207]\n"
    "v_mfma_f32_16x16x4_4b_bf16 v[16:31], a[242:243], a[248:249], v[16:31]\n"
    "v_mfma_f32_16x16x32_bf16 v[220:223], a[72:75], a[224:227], v[220:223]\n"
    "buffer_load_dwordx4 a[60:63], %10, %2, s23 offen offset:3072 sc1\n"
    "v_mfma_f32_16x16x32_bf16 v[220:223], a[76:79], a[228:231], v[220:223]\n"
    "v_mfma_f32_16x16x32_bf16 v[220:223], a[88:91], a[232:235], v[220:223]\n"
    "v_mfma_f32_16x16x32_bf16 v[220:223], a[92:95], a[236:239], v[220:223]\n"
    "s_waitcnt vmcnt(16) lgkmcnt(4)\n"
    "v_mfma_f32_16x16x32_bf16 v[192:195], a[96:99], a[192:195], v[192:195]\n"
    "s_add_u32 s22, s20, 49152\n"
    "ds_read_b128 a[224:227], %8 offset:45056\n"
    "ds_read_b128 a[228:231], %9 offset:45056\n"
    "v_mfma_f32_16x16x32_bf16 v[192:195], a[100:103], a[196:199], v[192:195]\n"
    "buffer_load_dwordx4 a[64:67], %10, %2, s22 offen offset:0 sc1\n"
    "ds_read_b128 a[232:235], %8 offset:61440\n"
    "v_mfma_f32_16x16x32_bf16 v[192:195], a[112:115], a[200:203], v[192:195]\n"
    "ds_read_b128 a[236:239], %9 offset:61440\n"
    "ds_read_u16 a240, %0 offset:128\n"
    "v_mfma_f32_16x16x32_bf16 v[192:195], a[116:119], a[204:207], v[192:195]\n"
    "ds_read_u16 a242, %0 offset:136\n"
    "v_mfma_f32_16x16x32_bf16 v[208:211], a[104:107], a[192:195], v[208:211]\n"
    "v_mfma_f32_16x16x32_bf16 v[208:211], a[108:111], a[196:199], v[208:211]\n"
    "buffer_load_dwordx4 a[68:71], %10, %2, s22 offen offset:1024 sc1\n"
    "v_mfma_f32_16x16x32_bf16 v[208:211], a[120:123], a[200:203], v[208:211]\n"
    "v_mfma_f32_16x16x32_bf16 v[208:211], a[124:127], a[204:207], v[208:211]\n"
    "s_waitcnt lgkmcnt(6)\n"
    "v_mfma_f32_16x16x32_bf16 v[196:199], a[96:99], a[208:211], v[196:199]\n"
    "ds_read_b128 a[192:195], %8 offset:47104\n"
    "ds_read_b128 a[196:199], %9 offset:47104\n"
    "v_mfma_f32_16x16x32_bf16 v[196:199], a[100:103], a[212:215], v[196:199]\n"
    "buffer_load_dwordx4 a[72:75], %10, %2, s22 offen offset:2048 sc1\n"
    "ds_read_b128 a[200:203], %8 offset:63488\n"
    "v_add_u32 v240, %5, %10\n"
    "v_mfma_f32_16x16x32_bf16 v[196:199], a[112:115], a[216:219], v[196:199]\n"
    "ds_read_b128 a[204:207], %9 offset:63488\n"
    "v_cvt_pk_bf16_f32 v224, v192, v208\n"
    "v_cvt_pk_bf16_f32 v225, v193, v209\n"
    "v_cvt_pk_bf16_f32 v226, v194, v210\n"
    "v_cvt_pk_bf16_f32 v227, v195, v211\n"
    "v_mfma_f32_16x16x32_bf16 v[196:199], a[116:119], a[220:223], v[196:199]\n"
    "ds_write_b128 v240, v[224:227] offset:0\n"
    "v_mfma_f32_16x16x32_bf16 v[212:215], a[104:107], a[208:211], v[212:215]\n"
    "v_mfma_f32_16x16x32_bf16 v[212:215], a[108:111], a[212:215], v[212:215]\n"
    "ds_write_b16 %1, a248 offset:64\n"
    "v_mfma_f32_16x16x32_bf16 v[212:215], a[120:123], a[216:219], v[212:215]\n"
    "v_mfma_f32_16x16x32_bf16 v[212:215], a[124:127], a[220:223], v[212:215]\n"
    "s_waitcnt lgkmcnt(6)\n"
    "buffer_load_dwordx4 a[76:79], %10, %2, s22 offen offset:3072 sc1\n"
    "v_mfma_f32_16x16x32_bf16 v[200:203], a[96:99], a[224:227], v[200:203]\n"
    "s_add_u32 s23, s20, 53248\n"
    "ds_read_u16 v242, %0 offset:6208\n"
    "v_mfma_f32_16x16x32_bf16 v[200:203], a[100:103], a[228:231], v[200:203]\n"
    "buffer_load_dwordx4 a[80:83], %10, %2, s23 offen offset:0 sc1\n"
    "s_nop 0\n"
    "v_cvt_pk_bf16_f32 v228, v196, v212\n"
    "v_mfma_f32_16x16x32_bf16 v[200:203], a[112:115], a[232:235], v[200:203]\n"
    "v_cvt_pk_bf16_f32 v229, v197, v213\n"
    "v_cvt_pk_bf16_f32 v230, v198, v214\n"
    "v_cvt_pk_bf16_f32 v231, v199, v215\n"
    "v_mfma_f32_16x16x32_bf16 v[200:203], a[116:119], a[236:239], v[200:203]\n"
    "ds_write_b128 v240, v[228:231] offset:1024\n"
    "s_waitcnt lgkmcnt(1)\n"
    "v_cmp_eq_u32 vcc, 0, v242\n"
    "v_mfma_f32_16x16x32_bf16 v[216:219], a[104:107], a[224:227], v[216:219]\n"
    "v_mfma_f32_16x16x32_bf16 v[216:219], a[108:111], a[228:231], v[216:219]\n"
    "s_cbranch_vccnz .Lslow2_%=\n"
    ".Lchk2_%=:\n"
    "ds_write_b16 %1, a248 offset:72\n"
    "v_mfma_f32_16x16x32_bf16 v[216:219], a[120:123], a[232:235], v[216:219]\n"
    "v_mfma_f32_16x16x32_bf16 v[216:219], a[124:127], a[236:239], v[216:219]\n"
    "buffer_load_dwordx4 a[84:87], %10, %2, s23 offen offset:1024 sc1\n"
    "v_mfma_f32_16x16x32_bf16 v[204:207], a[96:99], a[192:195], v[204:207]\n"
    "ds_read_b128 a[128:131], %10 offset:0\n"
    "ds_read_b128 a[144:147], %10 offset:4096\n"
    "v_mfma_f32_16x16x32_bf16 v[204:207], a[100:103], a[196:199], v[204:207]\n"
    "buffer_load_dwordx4 a[88:91], %10, %2, s23 offen offset:2048 sc1\n"
    "ds_read_u16 v242, %0 offset:6216\n"
    "s_nop 0\n"
    "v_cvt_pk_bf16_f32 v232, v200, v216\n"
    "v_mfma_f32_16x16x32_bf16 v[204:207], a[112:115], a[200:203], v[204:207]\n"
    "ds_read_b128 a[160:163], %10 offset:8192\n"
    "ds_read_b128 a[176:179], %10 offset:12288\n"
    "v_cvt_pk_bf16_f32 v233, v201, v217\n"
    "v_cvt_pk_bf16_f32 v234, v202, v218\n"
    "v_cvt_pk_bf16_f32 v235, v203, v219\n"
    "v_mfma_f32_16x16x32_bf16 v[204:207], a[116:119], a[204:207], v[204:207]\n"
    "ds_write_b128 v240, v[232:235] offset:2048\n"
    "v_mfma_f32_16x16x32_bf16 v[220:223], a[104:107], a[192:195], v[220:223]\n"
    "s_waitcnt lgkmcnt(3)\n"
    "v_cmp_eq_u32 vcc, 0, v242\n"
    "v_mfma_f32_16x16x32_bf16 v[220:223], a[108:111], a[196:199], v[220:223]\n"
    "ds_write_b16 %1, a248 offset:128\n"
    "v_lshlrev_b32 v228, 16, v130\n"
    "v_mfma_f32_16x16x32_bf16 v[220:223], a[120:123], a[200:203], v[220:223]\n"
    "s_cbranch_vccnz .Lslow3_%=\n"
    ".Lchk3_%=:\n"
    "v_and_b32 v229, s25, v130\n"
    "v_lshlrev_b32 v230, 16, v131\n"
    "v_and_b32 v231, s25, v131\n"
    "v_mfma_f32_16x16x32_bf16 v[220:223], a[124:127], a[204:207], v[220:223]\n"
    "buffer_load_dwordx4 a[92:95], %10, %2, s23 offen offset:3072 sc1\n"
    "s_waitcnt vmcnt(16)\n"
    "s_sub_u32 s24, s24, 1\n"
    "s_cmp_eq_u32 s24, 0\n"
    "s_cbranch_scc1 .Ldcn_tail%=\n"
    "v_mfma_f32_16x16x32_bf16 v[0:3], a[0:3], a[128:131], v[0:3]\n"
    "s_add_u32 s22, s20, 57344\n"
    "ds_read_b128 a[132:135], %10 offset:1024\n"
    "ds_read_b128 a[148:151], %10 offset:5120\n"
    "v_mfma_f32_16x16x32_bf16 v[0:3], a[4:7], a[144:147], v[0:3]\n"
    "s_waitcnt lgkmcnt(4)\n"
    "buffer_load_dwordx4 a[96:99], %10, %2, s22 offen offset:0 sc1\n"
    "ds_read_u16 v242, %0 offset:6272\n"
    "v_cvt_pk_bf16_f32 v236, v204, v220\n"
    "v_mfma_f32_16x16x32_bf16 v[0:3], a[16:19], a[160:163], v[0:3]\n"
    "ds_read_b128 a[164:167], %10 offset:9216\n"
    "ds_read_b128 a[180:183], %10 offset:13312\n"
    "v_cvt_pk_bf16_f32 v237, v205, v221\n"
    "v_cvt_pk_bf16_f32 v238, v206, v222\n"
    "v_cvt_pk_bf16_f32 v239, v207, v223\n"
    "v_mfma_f32_16x16x32_bf16 v[0:3], a[20:23], a[176:179], v[0:3]\n"
    "ds_write_b128 v240, v[236:239] offset:3072\n"
    "v_mfma_f32_16x16x32_bf16 v[16:19], a[8:11], a[128:131], v[16:19]\n"
    "s_waitcnt lgkmcnt(3)\n"
    "v_cmp_eq_u32 vcc, 0, v242\n"
    "v_mfma_f32_16x16x32_bf16 v[16:19], a[12:15], a[144:147], v[16:19]\n"
    "ds_write_b16 %1, a248 offset:136\n"
    "v_lshlrev_b32 v224, 16, v128\n"
    "v_and_b32 v225, s25, v128\n"
    "v_mfma_f32_16x16x32_bf16 v[16:19], a[24:27], a[160:163], v[16:19]\n"
    "s_cbranch_vccnz .Lslow4_%=\n"
    ".Lchk4_%=:\n"
    "v_lshlrev_b32 v226, 16, v129\n"
    "v_and_b32 v227, s25, v129\n"
    "v_lshlrev_b32 v236, 16, v134\n"
    "v_and_b32 v237, s25, v134\n"
    "v_mfma_f32_16x16x32_bf16 v[16:19], a[28:31], a[176:179], v[16:19]\n"
    "buffer_load_dwordx4 a[100:103], %10, %2, s22 offen offset:1024 sc1\n"
    "v_lshlrev_b32 v238, 16, v135\n"
    "v_and_b32 v239, s25, v135\n"
    "v_mfma_f32_16x16x32_bf16 v[4:7], a[0:3], a[132:135], v[4:7]\n"
    "ds_read_b128 a[136:139], %10 offset:2048\n"
    "ds_read_b128 a[152:155], %10 offset:6144\n"
    "v_lshlrev_b32 v232, 16, v132\n"
    "v_and_b32 v233, s25, v132\n"
    "v_lshlrev_b32 v234, 16, v133\n"
    "v_and_b32 v235, s25, v133\n"
    "v_pk_mul_f32 v[224:225], v[224:225], v[0:1]\n"
    "v_pk_mul_f32 v[226:227], v[226:227], v[2:3]\n"
    "v_pk_mul_f32 v[228:229], v[228:229], v[16:17]\n"
    "v_pk_mul_f32 v[230:231], v[230:231], v[18:19]\n"
    "v_mfma_f32_16x16x32_bf16 v[4:7], a[4:7], a[148:151], v[4:7]\n"
    "s_waitcnt lgkmcnt(2)\n"
    "ds_read_u16 v242, %0 offset:6280\n"
    "ds_read_b128 a[168:171], %10 offset:10240\n"
    "v_add_u32 v240, %4, %8\n"
    "v_cvt_pk_bf16_f32 v224, v224, v225\n"
    "v_mfma_f32_16x16x32_bf16 v[4:7], a[16:19], a[164:167], v[4:7]\n"
    "buffer_load_dwordx4 a[104:107], %10, %2, s22 offen offset:2048 sc1\n"
    "ds_read_b128 a[184:187], %10 offset:14336\n"
    "v_mfma_f32_16x16x32_bf16 v[4:7], a[20:23], a[180:183], v[4:7]\n"
    "v_cvt_pk_bf16_f32 v225, v226, v227\n"
    "v_cvt_pk_bf16_f32 v226, v228, v229\n"
    "v_cvt_pk_bf16_f32 v227, v230, v231\n"
    "v_mfma_f32_16x16x32_bf16 v[20:23], a[8:11], a[132:135], v[20:23]\n"
    "ds_write_b128 v240, v[224:227] offset:0\n"
    "s_waitcnt lgkmcnt(3)\n"
    "v_cmp_eq_u32 vcc, 0, v242\n"
    "v_mfma_f32_16x16x32_bf16 v[20:23], a[12:15], a[148:151], v[20:23]\n"
    "v_lshlrev_b32 v228, 16, v138\n"
    "v_and_b32 v229, s25, v138\n"
    "v_mfma_f32_16x16x32_bf16 v[20:23], a[24:27], a[164:167], v[20:23]\n"
    "s_cbranch_vccnz .Lslow5_%=\n"
    ".Lchk5_%=:\n"
    "v_lshlrev_b32 v230, 16, v139\n"
    "v_and_b32 v231, s25, v139\n"
    "v_mfma_f32_16x16x32_bf16 v[20:23], a[28:31], a[180:183], v[20:23]\n"
    "buffer_load_dwordx4 a[108:111], %10, %2, s22 offen offset:3072 sc1\n"
    "v_mfma_f32_16x16x32_bf16 v[8:11], a[0:3], a[136:139], v[8:11]\n"
    "ds_read_b128 a[140:143], %10 offset:3072\n"
    "ds_read_b128 a[156:159], %10 offset:7168\n"
    "v_lshlrev_b32 v224, 16, v136\n"
    "v_and_b32 v225, s25, v136\n"
    "v_lshlrev_b32 v226, 16, v137\n"
    "v_and_b32 v227, s25, v137\n"
    "v_pk_mul_f32 v[232:233], v[232:233], v[4:5]\n"
    "v_pk_mul_f32 v[234:235], v[234:235], v[6:7]\n"
    "v_pk_mul_f32 v[236:237], v[236:237], v[20:21]\n"
    "v_pk_mul_f32 v[238:239], v[238:239], v[22:23]\n"
    "v_mfma_f32_16x16x32_bf16 v[8:11], a[4:7], a[152:155], v[8:11]\n"
    "s_add_u32 s23, s20, 61440\n"
    "ds_read_b128 a[172:175], %10 offset:11264\n"
    "ds_read_b128 a[188:191], %10 offset:15360\n"
    "v_cvt_pk_bf16_f32 v232, v232, v233\n"
    "s_waitcnt lgkmcnt(4)\n"
    "v_mfma_f32_16x16x32_bf16 v[8:11], a[16:19], a[168:171], v[8:11]\n"
    "buffer_load_dwordx4 a[112:115], %10, %2, s23 offen offset:0 sc1\n"
    "v_mfma_f32_16x16x32_bf16 v[8:11], a[20:23], a[184:187], v[8:11]\n"
    "v_cvt_pk_bf16_f32 v233, v234, v235\n"
    "v_cvt_pk_bf16_f32 v234, v236, v237\n"
    "v_cvt_pk_bf16_f32 v235, v238, v239\n"
    "v_mfma_f32_16x16x4_4b_bf16 v[32:47], a[244:245], a[248:249], v[32:47]\n"
    "ds_write_b128 v240, v[232:235] offset:2048\n"
    "v_mfma_f32_16x16x32_bf16 v[24:27], a[8:11], a[136:139], v[24:27]\n"
    "v_lshlrev_b32 v236, 16, v142\n"
    "v_mfma_f32_16x16x32_bf16 v[24:27], a[12:15], a[152:155], v[24:27]\n"
    "v_and_b32 v237, s25, v142\n"
    "v_lshlrev_b32 v238, 16, v143\n"
    "v_mfma_f32_16x16x32_bf16 v[24:27], a[24:27], a[168:171], v[24:27]\n"
    "v_and_b32 v239, s25, v143\n"
    "v_mfma_f32_16x16x32_bf16 v[24:27], a[28:31], a[184:187], v[24:27]\n"
    "s_waitcnt lgkmcnt(3)\n"
    "buffer_load_dwordx4 a[116:119], %10, %2, s23 offen offset:1024 sc1\n"
    "v_mfma_f32_16x16x32_bf16 v[12:15], a[0:3], a[140:143], v[12:15]\n"
    "v_lshlrev_b32 v232, 16, v140\n"
    "v_and_b32 v233, s25, v140\n"
    "v_lshlrev_b32 v234, 16, v141\n"
    "v_and_b32 v235, s25, v141\n"
    "v_pk_mul_f32 v[224:225], v[224:225], v[8:9]\n"
    "v_pk_mul_f32 v[226:227], v[226:227], v[10:11]\n"
    "v_pk_mul_f32 v[228:229], v[228:229], v[24:25]\n"
    "v_pk_mul_f32 v[230:231], v[230:231], v[26:27]\n"
    "v_mfma_f32_16x16x32_bf16 v[12:15], a[4:7], a[156:159], v[12:15]\n"
    "s_waitcnt lgkmcnt(1)\n"
    "v_cvt_pk_bf16_f32 v224, v224, v225\n"
    "v_cvt_pk_bf16_f32 v225, v226, v227\n"
    "v_mfma_f32_16x16x32_bf16 v[12:15], a[16:19], a[172:175], v[12:15]\n"
    "buffer_load_dwordx4 a[120:123], %10, %2, s23 offen offset:2048 sc1\n"
    "v_mfma_f32_16x16x32_bf16 v[12:15], a[20:23], a[188:191], v[12:15]\n"
    "v_cvt_pk_bf16_f32 v226, v228, v229\n"
    "v_cvt_pk_bf16_f32 v227, v230, v231\n"
    "v_mfma_f32_16x16x4_4b_bf16 v[48:63], a[246:247], a[248:249], v[48:63]\n"
    "ds_write_b128 v240, v[224:227] offset:4096\n"
    "v_mfma_f32_16x16x32_bf16 v[28:31], a[8:11], a[140:143], v[28:31]\n"
    "v_lshlrev_b32 v228, 16, v146\n"
    "v_mfma_f32_16x16x32_bf16 v[28:31], a[12:15], a[156:159], v[28:31]\n"
    "v_and_b32 v229, s25, v146\n"
    "v_lshlrev_b32 v230, 16, v147\n"
    "v_mfma_f32_16x16x32_bf16 v[28:31], a[24:27], a[172:175], v[28:31]\n"
    "v_and_b32 v231, s25, v147\n"
    "v_mfma_f32_16x16x32_bf16 v[28:31], a[28:31], a[188:191], v[28:31]\n"
    "buffer_load_dwordx4 a[124:127], %10, %2, s23 offen offset:3072 sc1\n"
    "s_waitcnt vmcnt(16)\n"
    "v_mfma_f32_16x16x32_bf16 v[32:35], a[32:35], a[128:131], v[32:35]\n"
    "v_lshlrev_b32 v224, 16, v144\n"
    "v_and_b32 v225, s25, v144\n"
    "v_lshlrev_b32 v226, 16, v145\n"
    "v_and_b32 v227, s25, v145\n"
    "v_pk_mul_f32 v[232:233], v[232:233], v[12:13]\n"
    "v_pk_mul_f32 v[234:235], v[234:235], v[14:15]\n"
    "v_pk_mul_f32 v[236:237], v[236:237], v[28:29]\n"
    "v_pk_mul_f32 v[238:239], v[238:239], v[30:31]\n"
    "v_mfma_f32_16x16x32_bf16 v[32:35], a[36:39], a[144:147], v[32:35]\n"
    "s_add_u32 s22, s21, 0\n"
    "v_cvt_pk_bf16_f32 v232, v232, v233\n"
    "v_cvt_pk_bf16_f32 v233, v234, v235\n"
    "v_mfma_f32_16x16x32_bf16 v[32:35], a[48:51], a[160:163], v[32:35]\n"
    "buffer_load_dwordx4 a[0:3], %10, %2, s22 offen offset:0 sc1\n"
    "v_mfma_f32_16x16x32_bf16 v[32:35], a[52:55], a[176:179], v[32:35]\n"
    "v_cvt_pk_bf16_f32 v234, v236, v237\n"
    "v_cvt_pk_bf16_f32 v235, v238, v239\n"
    "v_mfma_f32_16x16x32_bf16 v[48:51], a[40:43], a[128:131], v[48:51]\n"
    "ds_write_b128 v240, v[232:235] offset:6144\n"
    "v_mfma_f32_16x16x32_bf16 v[48:51], a[44:47], a[144:147], v[48:51]\n"
    "v_lshlrev_b32 v236, 16, v150\n"
    "v_and_b32 v237, s25, v150\n"
    "v_mfma_f32_16x16x32_bf16 v[48:51], a[56:59], a[160:163], v[48:51]\n"
    "v_lshlrev_b32 v238, 16, v151\n"
    "v_and_b32 v239, s25, v151\n"
    "v_mfma_f32_16x16x32_bf16 v[48:51], a[60:63], a[176:179], v[48:51]\n"
    "buffer_load_dwordx4 a[4:7], %10, %2, s22 offen offset:1024 sc1\n"
    "v_mfma_f32_16x16x32_bf16 v[36:39], a[32:35], a[132:135], v[36:39]\n"
    "ds_read_u16 a244, %0 offset:192\n"
    "v_lshlrev_b32 v232, 16, v148\n"
    "v_and_b32 v233, s25, v148\n"
    "v_lshlrev_b32 v234, 16, v149\n"
    "v_and_b32 v235, s25, v149\n"
    "v_pk_mul_f32 v[224:225], v[224:225], v[32:33]\n"
    "v_pk_mul_f32 v[226:227], v[226:227], v[34:35]\n"
    "v_pk_mul_f32 v[228:229], v[228:229], v[48:49]\n"
    "v_pk_mul_f32 v[230:231], v[230:231], v[50:51]\n"
    "v_mfma_f32_16x16x32_bf16 v[36:39], a[36:39], a[148:151], v[36:39]\n"
    "v_add_u32 v241, %4, %9\n"
    "v_cvt_pk_bf16_f32 v224, v224, v225\n"
    "v_cvt_pk_bf16_f32 v225, v226, v227\n"
    "v_mfma_f32_16x16x32_bf16 v[36:39], a[48:51], a[164:167], v[36:39]\n"
    "buffer_load_dwordx4 a[8:11], %10, %2, s22 offen offset:2048 sc1\n"
    "v_mfma_f32_16x16x32_bf16 v[36:39], a[52:55], a[180:183], v[36:39]\n"
    "v_cvt_pk_bf16_f32 v226, v228, v229\n"
    "v_cvt_pk_bf16_f32 v227, v230, v231\n"
    "v_mfma_f32_16x16x4_4b_bf16 v[64:79], a[240:241], a[248:249], v[64:79]\n"
    "ds_write_b128 v241, v[224:227] offset:0\n"
    "v_mfma_f32_16x16x32_bf16 v[52:55], a[40:43], a[132:135], v[52:55]\n"
    "v_lshlrev_b32 v228, 16, v154\n"
    "v_mfma_f32_16x16x32_bf16 v[52:55], a[44:47], a[148:151], v[52:55]\n"
    "v_and_b32 v229, s25, v154\n"
    "v_lshlrev_b32 v230, 16, v155\n"
    "v_mfma_f32_16x16x32_bf16 v[52:55], a[56:59], a[164:167], v[52:55]\n"
    "v_and_b32 v231, s25, v155\n"
    "v_mfma_f32_16x16x32_bf16 v[52:55], a[60:63], a[180:183], v[52:55]\n"
    "buffer_load_dwordx4 a[12:15], %10, %2, s22 offen offset:3072 sc1\n"
    "v_mfma_f32_16x16x32_bf16 v[40:43], a[32:35], a[136:139], v[40:43]\n"
    "ds_read_u16 a246, %0 offset:200\n"
    "v_lshlrev_b32 v224, 16, v152\n"
    "v_and_b32 v225, s25, v152\n"
    "v_lshlrev_b32 v226, 16, v153\n"
    "v_and_b32 v227, s25, v153\n"
    "v_pk_mul_f32 v[232:233], v[232:233], v[36:37]\n"
    "v_pk_mul_f32 v[234:235], v[234:235], v[38:39]\n"
    "v_pk_mul_f32 v[236:237], v[236:237], v[52:53]\n"
    "v_pk_mul_f32 v[238:239], v[238:239], v[54:55]\n"
    "v_mfma_f32_16x16x32_bf16 v[40:43], a[36:39], a[152:155], v[40:43]\n"
    "s_add_u32 s23, s21, 4096\n"
    "v_cvt_pk_bf16_f32 v232, v232, v233\n"
    "v_cvt_pk_bf16_f32 v233, v234, v235\n"
    "v_mfma_f32_16x16x32_bf16 v[40:43], a[48:51], a[168:171], v[40:43]\n"
    "buffer_load_dwordx4 a[16:19], %10, %2, s23 offen offset:0 sc1\n"
    "v_mfma_f32_16x16x32_bf16 v[40:43], a[52:55], a[184:187], v[40:43]\n"
    "v_cvt_pk_bf16_f32 v234, v236, v237\n"
    "v_cvt_pk_bf16_f32 v235, v238, v239\n"
    "v_mfma_f32_16x16x4_4b_bf16 v[80:95], a[242:243], a[248:249], v[80:95]\n"
    "ds_write_b128 v241, v[232:235] offset:2048\n"
    "v_mfma_f32_16x16x32_bf16 v[56:59], a[40:43], a[136:139], v[56:59]\n"
    "v_lshlrev_b32 v236, 16, v158\n"
    "v_mfma_f32_16x16x32_bf16 v[56:59], a[44:47], a[152:155], v[56:59]\n"
    "v_and_b32 v237, s25, v158\n"
    "v_lshlrev_b32 v238, 16, v159\n"
    "v_mfma_f32_16x16x32_bf16 v[56:59], a[56:59], a[168:171], v[56:59]\n"
    "v_and_b32 v239, s25, v159\n"
    "v_mfma_f32_16x16x32_bf16 v[56:59], a[60:63], a[184:187], v[56:59]\n"
    "buffer_load_dwordx4 a[20:23], %10, %2, s23 offen offset:1024 sc1\n"
    "v_mfma_f32_16x16x32_bf16 v[44:47], a[32:35], a[140:143], v[44:47]\n"
    "v_lshlrev_b32 v232, 16, v156\n"
    "v_and_b32 v233, s25, v156\n"
    "v_lshlrev_b32 v234, 16, v157\n"
    "v_and_b32 v235, s25, v157\n"
    "v_pk_mul_f32 v[224:225], v[224:225], v[40:41]\n"
    "v_pk_mul_f32 v[226:227], v[226:227], v[42:43]\n"
    "v_pk_mul_f32 v[228:229], v[228:229], v[56:57]\n"
    "v_pk_mul_f32 v[230:231], v[230:231], v[58:59]\n"
    "v_mfma_f32_16x16x32_bf16 v[44:47], a[36:39], a[156:159], v[44:47]\n"
    "v_cvt_pk_bf16_f32 v224, v224, v225\n"
    "v_cvt_pk_bf16_f32 v225, v226, v227\n"
    "v_mfma_f32_16x16x32_bf16 v[44:47], a[48:51], a[172:175], v[44:47]\n"
    "buffer_load_dwordx4 a[24:27], %10, %2, s23 offen offset:2048 sc1\n"
    "v_mfma_f32_16x16x32_bf16 v[44:47], a[52:55], a[188:191], v[44:47]\n"
    "v_cvt_pk_bf16_f32 v226, v228, v229\n"
    "v_cvt_pk_bf16_f32 v227, v230, v231\n"
    "v_mfma_f32_16x16x32_bf16 v[60:63], a[40:43], a[140:143], v[60:63]\n"
    "ds_write_b128 v241, v[224:227] offset:4096\n"
    "v_mfma_f32_16x16x32_bf16 v[60:63], a[44:47], a[156:159], v[60:63]\n"
    "v_lshlrev_b32 v228, 16, v162\n"
    "v_and_b32 v229, s25, v162\n"
    "v_mfma_f32_16x16x32_bf16 v[60:63], a[56:59], a[172:175], v[60:63]\n"
    "v_lshlrev_b32 v230, 16, v163\n"
    "v_and_b32 v231, s25, v163\n"
    "v_mfma_f32_16x16x32_bf16 v[60:63], a[60:63], a[188:191], v[60:63]\n"
    "buffer_load_dwordx4 a[28:31], %10, %2, s23 offen offset:3072 sc1\n"
    "s_waitcnt vmcnt(16)\n"
    "v_mfma_f32_16x16x32_bf16 v[64:67], a[64:67], a[128:131], v[64:67]\n"
    "s_add_u32 s22, s21, 8192\n"
    "v_lshlrev_b32 v224, 16, v160\n"
    "v_and_b32 v225, s25, v160\n"
    "v_lshlrev_b32 v226, 16, v161\n"
    "v_and_b32 v227, s25, v161\n"
    "v_pk_mul_f32 v[232:233], v[232:233], v[44:45]\n"
    "v_pk_mul_f32 v[234:235], v[234:235], v[46:47]\n"
    "v_pk_mul_f32 v[236:237], v[236:237], v[60:61]\n"
    "v_pk_mul_f32 v[238:239], v[238:239], v[62:63]\n"
    "v_mfma_f32_16x16x32_bf16 v[64:67], a[68:71], a[144:147], v[64:67]\n"
    "buffer_load_dwordx4 a[32:35], %10, %2, s22 offen offset:0 sc1\n"
    "v_cvt_pk_bf16_f32 v232, v232, v233\n"
    "v_mfma_f32_16x16x32_bf16 v[64:67], a[80:83], a[160:163], v[64:67]\n"
    "v_cvt_pk_bf16_f32 v233, v234, v235\n"
    "v_cvt_pk_bf16_f32 v234, v236, v237\n"
    "v_cvt_pk_bf16_f32 v235, v238, v239\n"
    "v_mfma_f32_16x16x32_bf16 v[64:67], a[84:87], a[176:179], v[64:67]\n"
    "buffer_load_dwordx4 a[36:39], %10, %2, s22 offen offset:1024 sc1\n"
    "v_mfma_f32_16x16x32_bf16 v[80:83], a[72:75], a[128:131], v[80:83]\n"
    "ds_write_b128 v241, v[232:235] offset:6144\n"
    "v_mfma_f32_16x16x32_bf16 v[80:83], a[76:79], a[144:147], v[80:83]\n"
    "v_lshlrev_b32 v236, 16, v166\n"
    "v_and_b32 v237, s25, v166\n"
    "v_mfma_f32_16x16x32_bf16 v[80:83], a[88:91], a[160:163], v[80:83]\n"
    "v_lshlrev_b32 v238, 16, v167\n"
    "v_and_b32 v239, s25, v167\n"
    "v_mfma_f32_16x16x32_bf16 v[80:83], a[92:95], a[176:179], v[80:83]\n"
    "ds_write_b16 %1, a248 offset:0\n"
    "v_mfma_f32_16x16x32_bf16 v[68:71], a[64:67], a[132:135], v[68:71]\n"
    "v_lshlrev_b32 v232, 16, v164\n"
    "v_and_b32 v233, s25, v164\n"
    "v_lshlrev_b32 v234, 16, v165\n"
    "v_and_b32 v235, s25, v165\n"
    "v_pk_mul_f32 v[224:225], v[224:225], v[64:65]\n"
    "v_pk_mul_f32 v[226:227], v[226:227], v[66:67]\n"
    "v_pk_mul_f32 v[228:229], v[228:229], v[80:81]\n"
    "v_pk_mul_f32 v[230:231], v[230:231], v[82:83]\n"
    "v_mfma_f32_16x16x32_bf16 v[68:71], a[68:71], a[148:151], v[68:71]\n"
    "v_add_u32 v240, %4, %8\n"
    "v_cvt_pk_bf16_f32 v224, v224, v225\n"
    "v_cvt_pk_bf16_f32 v225, v226, v227\n"
    "v_mfma_f32_16x16x32_bf16 v[68:71], a[80:83], a[164:167], v[68:71]\n"
    "buffer_load_dwordx4 a[40:43], %10, %2, s22 offen offset:2048 sc1\n"
    "v_mfma_f32_16x16x32_bf16 v[68:71], a[84:87], a[180:183], v[68:71]\n"
    "s_waitcnt lgkmcnt(1)\n"
    "v_cvt_pk_bf16_f32 v226, v228, v229\n"
    "v_cvt_pk_bf16_f32 v227, v230, v231\n"
    "v_mfma_f32_16x16x4_4b_bf16 v[96:111], a[244:245], a[248:249], v[96:111]\n"
    "ds_write_b128 v240, v[224:227] offset:8192\n"
    "v_mfma_f32_16x16x32_bf16 v[84:87], a[72:75], a[132:135], v[84:87]\n"
    "v_lshlrev_b32 v228, 16, v170\n"
    "v_mfma_f32_16x16x32_bf16 v[84:87], a[76:79], a[148:151], v[84:87]\n"
    "v_and_b32 v229, s25, v170\n"
    "v_lshlrev_b32 v230, 16, v171\n"
    "v_mfma_f32_16x16x32_bf16 v[84:87], a[88:91], a[164:167], v[84:87]\n"
    "v_and_b32 v231, s25, v171\n"
    "v_mfma_f32_16x16x32_bf16 v[84:87], a[92:95], a[180:183], v[84:87]\n"
    "buffer_load_dwordx4 a[44:47], %10, %2, s22 offen offset:3072 sc1\n"
    "v_mfma_f32_16x16x32_bf16 v[72:75], a[64:67], a[136:139], v[72:75]\n"
    "v_lshlrev_b32 v224, 16, v168\n"
    "v_and_b32 v225, s25, v168\n"
    "v_lshlrev_b32 v226, 16, v169\n"
    "v_and_b32 v227, s25, v169\n"
    "v_pk_mul_f32 v[232:233], v[232:233], v[68:69]\n"
    "v_pk_mul_f32 v[234:235], v[234:235], v[70:71]\n"
    "v_pk_mul_f32 v[236:237], v[236:237], v[84:85]\n"
    "v_pk_mul_f32 v[238:239], v[238:239], v[86:87]\n"
    "v_mfma_f32_16x16x32_bf16 v[72:75], a[68:71], a[152:155], v[72:75]\n"
    "s_add_u32 s23, s21, 12288\n"
    "v_cvt_pk_bf16_f32 v232, v232, v233\n"
    "v_cvt_pk_bf16_f32 v233, v234, v235\n"
    "v_mfma_f32_16x16x32_bf16 v[72:75], a[80:83], a[168:171], v[72:75]\n"
    "buffer_load_dwordx4 a[48:51], %10, %2, s23 offen offset:0 sc1\n"
    "v_mfma_f32_16x16x32_bf16 v[72:75], a[84:87], a[184:187], v[72:75]\n"
    "v_cvt_pk_bf16_f32 v234, v236, v237\n"
    "v_cvt_pk_bf16_f32 v235, v238, v239\n"
    "v_mfma_f32_16x16x4_4b_bf16 v[112:127], a[246:247], a[248:249], v[112:127]\n"
    "ds_write_b128 v240, v[232:235] offset:10240\n"
    "v_mfma_f32_16x16x32_bf16 v[88:91], a[72:75], a[136:139], v[88:91]\n"
    "v_lshlrev_b32 v236, 16, v174\n"
    "v_mfma_f32_16x16x32_bf16 v[88:91], a[76:79], a[152:155], v[88:91]\n"
    "v_and_b32 v237, s25, v174\n"
    "v_lshlrev_b32 v238, 16, v175\n"
    "v_mfma_f32_16x16x32_bf16 v[88:91], a[88:91], a[168:171], v[88:91]\n"
    "v_and_b32 v239, s25, v175\n"
    "v_mfma_f32_16x16x32_bf16 v[88:91], a[92:95], a[184:187], v[88:91]\n"
    "buffer_load_dwordx4 a[52:55], %10, %2, s23 offen offset:1024 sc1\n"
    "v_mfma_f32_16x16x32_bf16 v[76:79], a[64:67], a[140:143], v[76:79]\n"
    "v_lshlrev_b32 v232, 16, v172\n"
    "v_and_b32 v233, s25, v172\n"
    "v_lshlrev_b32 v234, 16, v173\n"
    "v_and_b32 v235, s25, v173\n"
    "v_pk_mul_f32 v[224:225], v[224:225], v[72:73]\n"
    "v_pk_mul_f32 v[226:227], v[226:227], v[74:75]\n"
    "v_pk_mul_f32 v[228:229], v[228:229], v[88:89]\n"
    "v_pk_mul_f32 v[230:231], v[230:231], v[90:91]\n"
    "v_mfma_f32_16x16x32_bf16 v[76:79], a[68:71], a[156:159], v[76:79]\n"
    "v_cvt_pk_bf16_f32 v224, v224, v225\n"
    "v_cvt_pk_bf16_f32 v225, v226, v227\n"
    "v_mfma_f32_16x16x32_bf16 v[76:79], a[80:83], a[172:175], v[76:79]\n"
    "buffer_load_dwordx4 a[56:59], %10, %2, s23 offen offset:2048 sc1\n"
    "v_mfma_f32_16x16x32_bf16 v[76:79], a[84:87], a[188:191], v[76:79]\n"
    "v_cvt_pk_bf16_f32 v226, v228, v229\n"
    "v_cvt_pk_bf16_f32 v227, v230, v231\n"
    "v_mfma_f32_16x16x32_bf16 v[92:95], a[72:75], a[140:143], v[92:95]\n"
    "ds_write_b128 v240, v[224:227] offset:12288\n"
    "v_mfma_f32_16x16x32_bf16 v[92:95], a[76:79], a[156:159], v[92:95]\n"
    "v_lshlrev_b32 v228, 16, v178\n"
    "v_and_b32 v229, s25, v178\n"
    "v_mfma_f32_16x16x32_bf16 v[92:95], a[88:91], a[172:175], v[92:95]\n"
    "v_lshlrev_b32 v230, 16, v179\n"
    "v_and_b32 v231, s25, v179\n"
    "v_mfma_f32_16x16x32_bf16 v[92:95], a[92:95], a[188:191], v[92:95]\n"
    "buffer_load_dwordx4 a[60:63], %10, %2, s23 offen offset:3072 sc1\n"
    "s_waitcnt vmcnt(16)\n"
    "v_mfma_f32_16x16x32_bf16 v[96:99], a[96:99], a[128:131], v[96:99]\n"
    "v_lshlrev_b32 v224, 16, v176\n"
    "v_and_b32 v225, s25, v176\n"
    "v_lshlrev_b32 v226, 16, v177\n"
    "v_and_b32 v227, s25, v177\n"
    "v_pk_mul_f32 v[232:233], v[232:233], v[76:77]\n"
    "v_pk_mul_f32 v[234:235], v[234:235], v[78:79]\n"
    "v_pk_mul_f32 v[236:237], v[236:237], v[92:93]\n"
    "v_pk_mul_f32 v[238:239], v[238:239], v[94:95]\n"
    "v_mfma_f32_16x16x32_bf16 v[96:99], a[100:103], a[144:147], v[96:99]\n"
    "s_add_u32 s22, s21, 16384\n"
    "v_cvt_pk_bf16_f32 v232, v232, v233\n"
    "v_cvt_pk_bf16_f32 v233, v234, v235\n"
    "v_mfma_f32_16x16x32_bf16 v[96:99], a[112:115], a[160:163], v[96:99]\n"
    "buffer_load_dwordx4 a[64:67], %10, %2, s22 offen offset:0 sc1\n"
    "v_mfma_f32_16x16x32_bf16 v[96:99], a[116:119], a[176:179], v[96:99]\n"
    "v_cvt_pk_bf16_f32 v234, v236, v237\n"
    "v_cvt_pk_bf16_f32 v235, v238, v239\n"
    "v_mfma_f32_16x16x32_bf16 v[112:115], a[104:107], a[128:131], v[112:115]\n"
    "ds_write_b128 v240, v[232:235] offset:14336\n"
    "v_mfma_f32_16x16x32_bf16 v[112:115], a[108:111], a[144:147], v[112:115]\n"
    "v_lshlrev_b32 v236, 16, v182\n"
    "v_and_b32 v237, s25, v182\n"
    "v_mfma_f32_16x16x32_bf16 v[112:115], a[120:123], a[160:163], v[112:115]\n"
    "v_lshlrev_b32 v238, 16, v183\n"
    "v_and_b32 v239, s25, v183\n"
    "v_mfma_f32_16x16x32_bf16 v[112:115], a[124:127], a[176:179], v[112:115]\n"
    "buffer_load_dwordx4 a[68:71], %10, %2, s22 offen offset:1024 sc1\n"
    "v_mfma_f32_16x16x32_bf16 v[100:103], a[96:99], a[132:135], v[100:103]\n"
    "ds_read_u16 v242, %0 offset:6144\n"
    "v_lshlrev_b32 v232, 16, v180\n"
    "v_and_b32 v233, s25, v180\n"
    "v_lshlrev_b32 v234, 16, v181\n"
    "v_and_b32 v235, s25, v181\n"
    "v_pk_mul_f32 v[224:225], v[224:225], v[96:97]\n"
    "v_pk_mul_f32 v[226:227], v[226:227], v[98:99]\n"
    "v_pk_mul_f32 v[228:229], v[228:229], v[112:113]\n"
    "v_pk_mul_f32 v[230:231], v[230:231], v[114:115]\n"
    "v_mfma_f32_16x16x32_bf16 v[100:103], a[100:103], a[148:151], v[100:103]\n"
    "v_add_u32 v241, %4, %9\n"
    "v_cvt_pk_bf16_f32 v224, v224, v225\n"
    "v_cvt_pk_bf16_f32 v225, v226, v227\n"
    "v_mfma_f32_16x16x32_bf16 v[100:103], a[112:115], a[164:167], v[100:103]\n"
    "buffer_load_dwordx4 a[72:75], %10, %2, s22 offen offset:2048 sc1\n"
    "v_mfma_f32_16x16x32_bf16 v[100:103], a[116:119], a[180:183], v[100:103]\n"
    "s_waitcnt lgkmcnt(0)\n"
    "v_cvt_pk_bf16_f32 v226, v228, v229\n"
    "v_cvt_pk_bf16_f32 v227, v230, v231\n"
    "v_cmp_eq_u32 vcc, 0, v242\n"
    "v_mfma_f32_16x16x32_bf16 v[116:119], a[104:107], a[132:135], v[116:119]\n"
    "ds_write_b128 v241, v[224:227] offset:8192\n"
    "v_mfma_f32_16x16x32_bf16 v[116:119], a[108:111], a[148:151], v[116:119]\n"
    "s_cbranch_vccnz .Lslow0_%=\n"
    ".Lchk0_%=:\n"
    "v_lshlrev_b32 v228, 16, v186\n"
    "v_and_b32 v229, s25, v186\n"
    "v_mfma_f32_16x16x32_bf16 v[116:119], a[120:123], a[164:167], v[116:119]\n"
    "v_lshlrev_b32 v230, 16, v187\n"
    "v_and_b32 v231, s25, v187\n"
    "v_mfma_f32_16x16x32_bf16 v[116:119], a[124:127], a[180:183], v[116:119]\n"
    "buffer_load_dwordx4 a[76:79], %10, %2, s22 offen offset:3072 sc1\n"
    "v_mfma_f32_16x16x32_bf16 v[104:107], a[96:99], a[136:139], v[104:107]\n"
    "ds_read_b128 a[192:195], %8 offset:0\n"
    "ds_read_b128 a[196:199], %9 offset:0\n"
    "v_lshlrev_b32 v224, 16, v184\n"
    "v_and_b32 v225, s25, v184\n"
    "v_lshlrev_b32 v226, 16, v185\n"
    "v_and_b32 v227, s25, v185\n"
    "v_pk_mul_f32 v[232:233], v[232:233], v[100:101]\n"
    "v_pk_mul_f32 v[234:235], v[234:235], v[102:103]\n"
    "v_pk_mul_f32 v[236:237], v[236:237], v[116:117]\n"
    "v_pk_mul_f32 v[238:239], v[238:239], v[118:119]\n"
    "v_mfma_f32_16x16x32_bf16 v[104:107], a[100:103], a[152:155], v[104:107]\n"
    "s_add_u32 s23, s21, 20480\n"
    "ds_read_b128 a[200:203], %8 offset:16384\n"
    "ds_read_b128 a[204:207], %9 offset:16384\n"
    "v_cvt_pk_bf16_f32 v232, v232, v233\n"
    "v_mfma_f32_16x16x32_bf16 v[104:107], a[112:115], a[168:171], v[104:107]\n"
    "buffer_load_dwordx4 a[80:83], %10, %2, s23 offen offset:0 sc1\n"
    "v_mfma_f32_16x16x32_bf16 v[104:107], a[116:119], a[184:187], v[104:107]\n"
    "v_cvt_pk_bf16_f32 v233, v234, v235\n"
    "v_cvt_pk_bf16_f32 v234, v236, v237\n"
    "v_cvt_pk_bf16_f32 v235, v238, v239\n"
    "v_mfma_f32_16x16x32_bf16 v[120:123], a[104:107], a[136:139], v[120:123]\n"
    "ds_write_b128 v241, v[232:235] offset:10240\n"
    "v_mfma_f32_16x16x32_bf16 v[120:123], a[108:111], a[152:155], v[120:123]\n"
    "v_lshlrev_b32 v236, 16, v190\n"
    "v_and_b32 v237, s25, v190\n"
    "v_mfma_f32_16x16x32_bf16 v[120:123], a[120:123], a[168:171], v[120:123]\n"
    "v_lshlrev_b32 v238, 16, v191\n"
    "v_and_b32 v239, s25, v191\n"
    "v_mfma_f32_16x16x32_bf16 v[120:123], a[124:127], a[184:187], v[120:123]\n"
    "buffer_load_dwordx4 a[84:87], %10, %2, s23 offen offset:1024 sc1\n"
    "v_mfma_f32_16x16x32_bf16 v[108:111], a[96:99], a[140:143], v[108:111]\n"
    "ds_read_b128 a[208:211], %8 offset:2048\n"
    "ds_read_b128 a[212:215], %9 offset:2048\n"
    "v_lshlrev_b32 v232, 16, v188\n"
    "v_and_b32 v233, s25, v188\n"
    "v_lshlrev_b32 v234, 16, v189\n"
    "v_and_b32 v235, s25, v189\n"
    "v_pk_mul_f32 v[224:225], v[224:225], v[104:105]\n"
    "v_pk_mul_f32 v[226:227], v[226:227], v[106:107]\n"
    "v_pk_mul_f32 v[228:229], v[228:229], v[120:121]\n"
    "v_pk_mul_f32 v[230:231], v[230:231], v[122:123]\n"
    "v_mfma_f32_16x16x32_bf16 v[108:111], a[100:103], a[156:159], v[108:111]\n"
    "ds_read_b128 a[216:219], %8 offset:18432\n"
    "ds_read_b128 a[220:223], %9 offset:18432\n"
    "v_cvt_pk_bf16_f32 v224, v224, v225\n"
    "v_mfma_f32_16x16x32_bf16 v[108:111], a[112:115], a[172:175], v[108:111]\n"
    "buffer_load_dwordx4 a[88:91], %10, %2, s23 offen offset:2048 sc1\n"
    "v_mfma_f32_16x16x32_bf16 v[108:111], a[116:119], a[188:191], v[108:111]\n"
    "v_cvt_pk_bf16_f32 v225, v226, v227\n"
    "v_cvt_pk_bf16_f32 v226, v228, v229\n"
    "v_cvt_pk_bf16_f32 v227, v230, v231\n"
    "v_mfma_f32_16x16x32_bf16 v[124:127], a[104:107], a[140:143], v[124:127]\n"
    "ds_write_b128 v241, v[224:227] offset:12288\n"
    "v_mfma_f32_16x16x32_bf16 v[124:127], a[108:111], a[156:159], v[124:127]\n"
    "v_mfma_f32_16x16x32_bf16 v[124:127], a[120:123], a[172:175], v[124:127]\n"
    "v_mfma_f32_16x16x32_bf16 v[124:127], a[124:127], a[188:191], v[124:127]\n"
    "buffer_load_dwordx4 a[92:95], %10, %2, s23 offen offset:3072 sc1\n"
    "s_waitcnt vmcnt(16)\n"
    "v_add_u32 %0, 256, %0\n"
    "v_add_u32 %1, 256, %1\n"
    "s_mov_b32 s20, s21\n"
    "s_add_u32 s21, s21, 262144\n"
    "s_branch .Ldcn_loop%=\n"
    ".Lslow1f_%=:\n"
    "ds_read_u16 v242, %0 offset:6152\n"
    "s_waitcnt lgkmcnt(0)\n"
    "v_cmp_eq_u32 vcc, 0, v242\n"
    "s_cbranch_vccnz .Lslow1f_%=\n"
    "s_branch .Lchk1f_%=\n"
    ".Lslow6f_%=:\n"
    "ds_read_u16 v242, %0 offset:6336\n"
    "s_waitcnt lgkmcnt(0)\n"
    "v_cmp_eq_u32 vcc, 0, v242\n"
    "s_cbranch_vccnz .Lslow6f_%=\n"
    "s_branch .Lchk6f_%=\n"
    ".Lslow2f_%=:\n"
    "ds_read_u16 v242, %0 offset:6208\n"
    "s_waitcnt lgkmcnt(0)\n"
    "v_cmp_eq_u32 vcc, 0, v242\n"
    "s_cbranch_vccnz .Lslow2f_%=\n"
    "s_branch .Lchk2f_%=\n"
    ".Lslow3f_%=:\n"
    "ds_read_u16 v242, %0 offset:6216\n"
    "s_waitcnt lgkmcnt(0)\n"
    "v_cmp_eq_u32 vcc, 0, v242\n"
    "s_cbranch_vccnz .Lslow3f_%=\n"
    "s_branch .Lchk3f_%=\n"
    ".Lslow4f_%=:\n"
    "ds_read_u16 v242, %0 offset:6272\n"
    "s_waitcnt lgkmcnt(0)\n"
    "v_cmp_eq_u32 vcc, 0, v242\n"
    "s_cbranch_vccnz .Lslow4f_%=\n"
    "s_branch .Lchk4f_%=\n"
    ".Lslow5f_%=:\n"
    "ds_read_u16 v242, %0 offset:6280\n"
    "s_waitcnt lgkmcnt(0)\n"
    "v_cmp_eq_u32 vcc, 0, v242\n"
    "s_cbranch_vccnz .Lslow5f_%=\n"
    "s_branch .Lchk5f_%=\n"
    ".Lpoll%=:\n"
    "s_sleep 8\n"
    "buffer_load_dwordx2 v[192:193], v196, %15, 0 offen sc1\n"
    "s_sub_u32 s29, s29, 1\n"
    "s_waitcnt vmcnt(0)\n"
    "s_cmp_eq_u32 s29, 0\n"
    "s_cbranch_scc1 .Lpoll_e%=\n"
    "v_cmp_ne_u64 vcc, v[192:193], v[194:195]\n"
    "s_cbranch_vccnz .Lpoll%=\n"
    "s_branch .Lpoll_e%=\n"
    ".Lslow0f_%=:\n"
    "ds_read_u16 v242, %0 offset:6144\n"
    "s_waitcnt lgkmcnt(0)\n"
    "v_cmp_eq_u32 vcc, 0, v242\n"
    "s_cbranch_vccnz .Lslow0f_%=\n"
    "s_branch .Lchk0f_%=\n"
    ".Lslow1_%=:\n"
    "ds_read_u16 v242, %0 offset:6152\n"
    "s_waitcnt lgkmcnt(0)\n"
    "v_cmp_eq_u32 vcc, 0, v242\n"
    "s_cbranch_vccnz .Lslow1_%=\n"
    "s_branch .Lchk1_%=\n"
    ".Lslow2_%=:\n"
    "ds_read_u16 v242, %0 offset:6208\n"
    "s_waitcnt lgkmcnt(0)\n"
    "v_cmp_eq_u32 vcc, 0, v242\n"
    "s_cbranch_vccnz .Lslow2_%=\n"
    "s_branch .Lchk2_%=\n"
    ".Lslow3_%=:\n"
    "ds_read_u16 v242, %0 offset:6216\n"
    "s_waitcnt lgkmcnt(0)\n"
    "v_cmp_eq_u32 vcc, 0, v242\n"
    "s_cbranch_vccnz .Lslow3_%=\n"
    "s_branch .Lchk3_%=\n"
    ".Lslow4_%=:\n"
    "ds_read_u16 v242, %0 offset:6272\n"
    "s_waitcnt lgkmcnt(0)\n"
    "v_cmp_eq_u32 vcc, 0, v242\n"
    "s_cbranch_vccnz .Lslow4_%=\n"
    "s_branch .Lchk4_%=\n"
    ".Lslow5_%=:\n"
    "ds_read_u16 v242, %0 offset:6280\n"
    "s_waitcnt lgkmcnt(0)\n"
    "v_cmp_eq_u32 vcc, 0, v242\n"
    "s_cbranch_vccnz .Lslow5_%=\n"
    "s_branch .Lchk5_%=\n"
    ".Lslow0_%=:\n"
    "ds_read_u16 v242, %0 offset:6144\n"
    "s_waitcnt lgkmcnt(0)\n"
    "v_cmp_eq_u32 vcc, 0, v242\n"
    "s_cbranch_vccnz .Lslow0_%=\n"
    "s_branch .Lchk0_%=\n"
    ".Lslow4t_%=:\n"
    "ds_read_u16 v242, %0 offset:6272\n"
    "s_waitcnt lgkmcnt(0)\n"
    "v_cmp_eq_u32 vcc, 0, v242\n"
    "s_cbranch_vccnz .Lslow4t_%=\n"
    "s_branch .Lchk4t_%=\n"
    ".Lslow5t_%=:\n"
    "ds_read_u16 v242, %0 offset:6280\n"
    "s_waitcnt lgkmcnt(0)\n"
    "v_cmp_eq_u32 vcc, 0, v242\n"
    "s_cbranch_vccnz .Lslow5t_%=\n"
    "s_branch .Lchk5t_%=\n"
    ".Ldcn_tail%=:\n"
    "s_waitcnt vmcnt(8)\n"
    "s_mov_b64 exec, 1\n"
    "buffer_store_dwordx2 a[252:253], off, %16, 0 sc1\n"
    "s_mov_b64 exec, -1\n"
    "s_nop 3\n"
    "v_mfma_f32_16x16x32_bf16 v[0:3], a[0:3], a[128:131], v[0:3]\n"
    "s_add_u32 s22, s20, 57344\n"
    "ds_read_b128 a[132:135], %10 offset:1024\n"
    "ds_read_b128 a[148:151], %10 offset:5120\n"
    "v_mfma_f32_16x16x32_bf16 v[0:3], a[4:7], a[144:147], v[0:3]\n"
    "s_waitcnt lgkmcnt(3)\n"
    "buffer_load_dwordx4 a[96:99], %10, %2, s22 offen offset:0 sc1\n"
    "ds_read_u16 v242, %0 offset:6272\n"
    "v_cvt_pk_bf16_f32 v236, v204, v220\n"
    "v_mfma_f32_16x16x32_bf16 v[0:3], a[16:19], a[160:163], v[0:3]\n"
    "ds_read_b128 a[164:167], %10 offset:9216\n"
    "ds_read_b128 a[180:183], %10 offset:13312\n"
    "v_cvt_pk_bf16_f32 v237, v205, v221\n"
    "v_cvt_pk_bf16_f32 v238, v206, v222\n"
    "v_cvt_pk_bf16_f32 v239, v207, v223\n"
    "v_mfma_f32_16x16x32_bf16 v[0:3], a[20:23], a[176:179], v[0:3]\n"
    "ds_write_b128 v240, v[236:239] offset:3072\n"
    "v_mfma_f32_16x16x4_4b_bf16 v[32:47], a[244:245], a[248:249], v[32:47]\n"
    "s_waitcnt lgkmcnt(3)\n"
    "v_cmp_eq_u32 vcc, 0, v242\n"
    "v_mfma_f32_16x16x4_4b_bf16 v[48:63], a[246:247], a[248:249], v[48:63]\n"
    "v_mfma_f32_16x16x32_bf16 v[16:19], a[8:11], a[128:131], v[16:19]\n"
    "s_cbranch_vccnz .Lslow4t_%=\n"
    ".Lchk4t_%=:\n"
    "v_mfma_f32_16x16x32_bf16 v[16:19], a[12:15], a[144:147], v[16:19]\n"
    "ds_write_b16 %1, a248 offset:136\n"
    "v_lshlrev_b32 v224, 16, v128\n"
    "v_and_b32 v225, s25, v128\n"
    "v_mfma_f32_16x16x32_bf16 v[16:19], a[24:27], a[160:163], v[16:19]\n"
    "v_lshlrev_b32 v226, 16, v129\n"
    "v_and_b32 v227, s25, v129\n"
    "v_lshlrev_b32 v236, 16, v146\n"
    "v_and_b32 v237, s25, v146\n"
    "v_mfma_f32_16x16x32_bf16 v[16:19], a[28:31], a[176:179], v[16:19]\n"
    "buffer_load_dwordx4 a[100:103], %10, %2, s22 offen offset:1024 sc1\n"
    "v_lshlrev_b32 v238, 16, v147\n"
    "v_and_b32 v239, s25, v147\n"
    "v_mfma_f32_16x16x32_bf16 v[32:35], a[32:35], a[128:131], v[32:35]\n"
    "ds_read_b128 a[136:139], %10 offset:2048\n"
    "ds_read_b128 a[152:155], %10 offset:6144\n"
    "v_lshlrev_b32 v232, 16, v144\n"
    "v_and_b32 v233, s25, v144\n"
    "v_lshlrev_b32 v234, 16, v145\n"
    "v_and_b32 v235, s25, v145\n"
    "v_pk_mul_f32 v[224:225], v[224:225], v[0:1]\n"
    "v_pk_mul_f32 v[226:227], v[226:227], v[2:3]\n"
    "v_pk_mul_f32 v[228:229], v[228:229], v[16:17]\n"
    "v_pk_mul_f32 v[230:231], v[230:231], v[18:19]\n"
    "v_mfma_f32_16x16x32_bf16 v[32:35], a[36:39], a[144:147], v[32:35]\n"
    "ds_read_u16 v242, %0 offset:6280\n"
    "ds_read_b128 a[168:171], %10 offset:10240\n"
    "v_add_u32 v240, %4, %8\n"
    "v_cvt_pk_bf16_f32 v224, v224, v225\n"
    "v_mfma_f32_16x16x32_bf16 v[32:35], a[48:51], a[160:163], v[32:35]\n"
    "buffer_load_dwordx4 a[104:107], %10, %2, s22 offen offset:2048 sc1\n"
    "ds_read_b128 a[184:187], %10 offset:14336\n"
    "v_mfma_f32_16x16x32_bf16 v[32:35], a[52:55], a[176:179], v[32:35]\n"
    "v_cvt_pk_bf16_f32 v225, v226, v227\n"
    "v_cvt_pk_bf16_f32 v226, v228, v229\n"
    "v_cvt_pk_bf16_f32 v227, v230, v231\n"
    "v_mfma_f32_16x16x32_bf16 v[48:51], a[40:43], a[128:131], v[48:51]\n"
    "ds_write_b128 v240, v[224:227] offset:0\n"
    "s_waitcnt lgkmcnt(3)\n"
    "v_cmp_eq_u32 vcc, 0, v242\n"
    "v_mfma_f32_16x16x32_bf16 v[48:51], a[44:47], a[144:147], v[48:51]\n"
    "v_lshlrev_b32 v228, 16, v134\n"
    "v_and_b32 v229, s25, v134\n"
    "v_mfma_f32_16x16x32_bf16 v[48:51], a[56:59], a[160:163], v[48:51]\n"
    "s_cbranch_vccnz .Lslow5t_%=\n"
    ".Lchk5t_%=:\n"
    "v_lshlrev_b32 v230, 16, v135\n"
    "v_and_b32 v231, s25, v135\n"
    "v_mfma_f32_16x16x32_bf16 v[48:51], a[60:63], a[176:179], v[48:51]\n"
    "buffer_load_dwordx4 a[108:111], %10, %2, s22 offen offset:3072 sc1\n"
    "s_sub_u32 s28, %4, 65536\n"
    "v_add_u32 v192, s28, %10\n"
    "v_mfma_f32_16x16x32_bf16 v[4:7], a[0:3], a[132:135], v[4:7]\n"
    "ds_read_b128 a[140:143], %10 offset:3072\n"
    "ds_read_b128 a[156:159], %10 offset:7168\n"
    "v_lshlrev_b32 v224, 16, v132\n"
    "v_and_b32 v225, s25, v132\n"
    "v_lshlrev_b32 v226, 16, v133\n"
    "v_and_b32 v227, s25, v133\n"
    "v_pk_mul_f32 v[232:233], v[232:233], v[32:33]\n"
    "v_pk_mul_f32 v[234:235], v[234:235], v[34:35]\n"
    "v_pk_mul_f32 v[236:237], v[236:237], v[48:49]\n"
    "v_pk_mul_f32 v[238:239], v[238:239], v[50:51]\n"
    "v_mfma_f32_16x16x32_bf16 v[4:7], a[4:7], a[148:151], v[4:7]\n"
    "s_add_u32 s23, s20, 61440\n"
    "ds_read_b128 a[172:175], %10 offset:11264\n"
    "ds_read_b128 a[188:191], %10 offset:15360\n"
    "v_add_u32 v241, %4, %9\n"
    "v_cvt_pk_bf16_f32 v232, v232, v233\n"
    "v_mfma_f32_16x16x32_bf16 v[4:7], a[16:19], a[164:167], v[4:7]\n"
    "buffer_load_dwordx4 a[112:115], %10, %2, s23 offen offset:0 sc1\n"
    "ds_read_u16 a244, %0 offset:192\n"
    "v_mfma_f32_16x16x32_bf16 v[4:7], a[20:23], a[180:183], v[4:7]\n"
    "ds_read_u16 a246, %0 offset:200\n"
    "v_cvt_pk_bf16_f32 v233, v234, v235\n"
    "v_cvt_pk_bf16_f32 v234, v236, v237\n"
    "v_cvt_pk_bf16_f32 v235, v238, v239\n"
    "v_mfma_f32_16x16x32_bf16 v[20:23], a[8:11], a[132:135], v[20:23]\n"
    "ds_write_b128 v241, v[232:235] offset:0\n"
    "v_mfma_f32_16x16x32_bf16 v[20:23], a[12:15], a[148:151], v[20:23]\n"
    "v_lshlrev_b32 v236, 16, v150\n"
    "v_and_b32 v237, s25, v150\n"
    "v_mfma_f32_16x16x32_bf16 v[20:23], a[24:27], a[164:167], v[20:23]\n"
    "v_lshlrev_b32 v238, 16, v151\n"
    "v_and_b32 v239, s25, v151\n"
    "v_mfma_f32_16x16x32_bf16 v[20:23], a[28:31], a[180:183], v[20:23]\n"
    "buffer_load_dwordx4 a[116:119], %10, %2, s23 offen offset:1024 sc1\n"
    "v_mfma_f32_16x16x32_bf16 v[36:39], a[32:35], a[132:135], v[36:39]\n"
    "ds_read_b128 a[192:195], v192 offset:0\n"
    "ds_read_b128 a[196:199], v192 offset:1024\n"
    "v_lshlrev_b32 v232, 16, v148\n"
    "v_and_b32 v233, s25, v148\n"
    "v_lshlrev_b32 v234, 16, v149\n"
    "v_and_b32 v235, s25, v149\n"
    "v_pk_mul_f32 v[224:225], v[224:225], v[4:5]\n"
    "v_pk_mul_f32 v[226:227], v[226:227], v[6:7]\n"
    "v_pk_mul_f32 v[228:229], v[228:229], v[20:21]\n"
    "v_pk_mul_f32 v[230:231], v[230:231], v[22:23]\n"
    "v_mfma_f32_16x16x32_bf16 v[36:39], a[36:39], a[148:151], v[36:39]\n"
    "v_cvt_pk_bf16_f32 v224, v224, v225\n"
    "v_cvt_pk_bf16_f32 v225, v226, v227\n"
    "v_mfma_f32_16x16x32_bf16 v[36:39], a[48:51], a[164:167], v[36:39]\n"
    "buffer_load_dwordx4 a[120:123], %10, %2, s23 offen offset:2048 sc1\n"
    "v_mfma_f32_16x16x32_bf16 v[36:39], a[52:55], a[180:183], v[36:39]\n"
    "v_cvt_pk_bf16_f32 v226, v228, v229\n"
    "v_cvt_pk_bf16_f32 v227, v230, v231\n"
    "v_mfma_f32_16x16x4_4b_bf16 v[64:79], a[240:241], a[248:249], v[64:79]\n"
    "ds_write_b128 v240, v[224:227] offset:2048\n"
    "v_mfma_f32_16x16x32_bf16 v[52:55], a[40:43], a[132:135], v[52:55]\n"
    "v_lshlrev_b32 v228, 16, v138\n"
    "v_mfma_f32_16x16x32_bf16 v[52:55], a[44:47], a[148:151], v[52:55]\n"
    "v_and_b32 v229, s25, v138\n"
    "v_lshlrev_b32 v230, 16, v139\n"
    "v_mfma_f32_16x16x32_bf16 v[52:55], a[56:59], a[164:167], v[52:55]\n"
    "v_and_b32 v231, s25, v139\n"
    "v_mfma_f32_16x16x32_bf16 v[52:55], a[60:63], a[180:183], v[52:55]\n"
    "buffer_load_dwordx4 a[124:127], %10, %2, s23 offen offset:3072 sc1\n"
    "v_mfma_f32_16x16x32_bf16 v[8:11], a[0:3], a[136:139], v[8:11]\n"
    "s_waitcnt lgkmcnt(1)\n"
    "v_lshlrev_b32 v224, 16, v136\n"
    "v_and_b32 v225, s25, v136\n"
    "v_lshlrev_b32 v226, 16, v137\n"
    "v_and_b32 v227, s25, v137\n"
    "v_pk_mul_f32 v[232:233], v[232:233], v[36:37]\n"
    "v_pk_mul_f32 v[234:235], v[234:235], v[38:39]\n"
    "v_pk_mul_f32 v[236:237], v[236:237], v[52:53]\n"
    "v_pk_mul_f32 v[238:239], v[238:239], v[54:55]\n"
    "v_mfma_f32_16x16x32_bf16 v[8:11], a[4:7], a[152:155], v[8:11]\n"
    "s_mov_b32 s22, 0\n"
    "v_cvt_pk_bf16_f32 v232, v232, v233\n"
    "v_cvt_pk_bf16_f32 v233, v234, v235\n"
    "v_mfma_f32_16x16x32_bf16 v[8:11], a[16:19], a[168:171], v[8:11]\n"
    "buffer_store_dwordx4 a[192:195], %11, %3, s22 offen sc1\n"
    "v_mfma_f32_16x16x32_bf16 v[8:11], a[20:23], a[184:187], v[8:11]\n"
    "v_cvt_pk_bf16_f32 v234, v236, v237\n"
    "v_cvt_pk_bf16_f32 v235, v238, v239\n"
    "v_mfma_f32_16x16x4_4b_bf16 v[80:95], a[242:243], a[248:249], v[80:95]\n"
    "ds_write_b128 v241, v[232:235] offset:2048\n"
    "v_mfma_f32_16x16x32_bf16 v[24:27], a[8:11], a[136:139], v[24:27]\n"
    "v_lshlrev_b32 v236, 16, v154\n"
    "v_mfma_f32_16x16x32_bf16 v[24:27], a[12:15], a[152:155], v[24:27]\n"
    "v_and_b32 v237, s25, v154\n"
    "v_mfma_f32_16x16x32_bf16 v[24:27], a[24:27], a[168:171], v[24:27]\n"
    "v_lshlrev_b32 v238, 16, v155\n"
    "v_mfma_f32_16x16x32_bf16 v[24:27], a[28:31], a[184:187], v[24:27]\n"
    "v_and_b32 v239, s25, v155\n"
    "v_mfma_f32_16x16x32_bf16 v[40:43], a[32:35], a[136:139], v[40:43]\n"
    "ds_read_b128 a[200:203], v192 offset:2048\n"
    "ds_read_b128 a[204:207], v192 offset:3072\n"
    "v_lshlrev_b32 v232, 16, v152\n"
    "v_and_b32 v233, s25, v152\n"
    "v_lshlrev_b32 v234, 16, v153\n"
    "v_and_b32 v235, s25, v153\n"
    "v_pk_mul_f32 v[224:225], v[224:225], v[8:9]\n"
    "v_pk_mul_f32 v[226:227], v[226:227], v[10:11]\n"
    "v_pk_mul_f32 v[228:229], v[228:229], v[24:25]\n"
    "v_pk_mul_f32 v[230:231], v[230:231], v[26:27]\n"
    "v_mfma_f32_16x16x32_bf16 v[40:43], a[36:39], a[152:155], v[40:43]\n"
    "s_mov_b32 s23, 8192\n"
    "v_cvt_pk_bf16_f32 v224, v224, v225\n"
    "v_cvt_pk_bf16_f32 v225, v226, v227\n"
    "v_mfma_f32_16x16x32_bf16 v[40:43], a[48:51], a[168:171], v[40:43]\n"
    "buffer_store_dwordx4 a[196:199], %11, %3, s23 offen sc1\n"
    "v_mfma_f32_16x16x32_bf16 v[40:43], a[52:55], a[184:187], v[40:43]\n"
    "v_cvt_pk_bf16_f32 v226, v228, v229\n"
    "v_cvt_pk_bf16_f32 v227, v230, v231\n"
    "v_mfma_f32_16x16x4_4b_bf16 v[96:111], a[244:245], a[248:249], v[96:111]\n"
    "ds_write_b128 v240, v[224:227] offset:4096\n"
    "v_mfma_f32_16x16x32_bf16 v[56:59], a[40:43], a[136:139], v[56:59]\n"
    "v_lshlrev_b32 v228, 16, v142\n"
    "v_mfma_f32_16x16x32_bf16 v[56:59], a[44:47], a[152:155], v[56:59]\n"
    "v_and_b32 v229, s25, v142\n"
    "v_mfma_f32_16x16x32_bf16 v[56:59], a[56:59], a[168:171], v[56:59]\n"
    "v_lshlrev_b32 v230, 16, v143\n"
    "v_mfma_f32_16x16x32_bf16 v[56:59], a[60:63], a[184:187], v[56:59]\n"
    "v_and_b32 v231, s25, v143\n"
    "v_mfma_f32_16x16x32_bf16 v[12:15], a[0:3], a[140:143], v[12:15]\n"
    "v_lshlrev_b32 v224, 16, v140\n"
    "v_and_b32 v225, s25, v140\n"
    "v_lshlrev_b32 v226, 16, v141\n"
    "v_and_b32 v227, s25, v141\n"
    "v_pk_mul_f32 v[232:233], v[232:233], v[40:41]\n"
    "v_pk_mul_f32 v[234:235], v[234:235], v[42:43]\n"
    "v_pk_mul_f32 v[236:237], v[236:237], v[56:57]\n"
    "v_pk_mul_f32 v[238:239], v[238:239], v[58:59]\n"
    "v_mfma_f32_16x16x32_bf16 v[12:15], a[4:7], a[156:159], v[12:15]\n"
    "s_mov_b32 s22, 16384\n"
    "v_cvt_pk_bf16_f32 v232, v232, v233\n"
    "v_cvt_pk_bf16_f32 v233, v234, v235\n"
    "v_mfma_f32_16x16x32_bf16 v[12:15], a[16:19], a[172:175], v[12:15]\n"
    "s_waitcnt lgkmcnt(1)\n"
    "buffer_store_dwordx4 a[200:203], %11, %3, s22 offen sc1\n"
    "v_mfma_f32_16x16x32_bf16 v[12:15], a[20:23], a[188:191], v[12:15]\n"
    "v_cvt_pk_bf16_f32 v234, v236, v237\n"
    "v_cvt_pk_bf16_f32 v235, v238, v239\n"
    "v_mfma_f32_16x16x4_4b_bf16 v[112:127], a[246:247], a[248:249], v[112:127]\n"
    "ds_write_b128 v241, v[232:235] offset:4096\n"
    "v_mfma_f32_16x16x32_bf16 v[28:31], a[8:11], a[140:143], v[28:31]\n"
    "v_lshlrev_b32 v236, 16, v158\n"
    "v_mfma_f32_16x16x32_bf16 v[28:31], a[12:15], a[156:159], v[28:31]\n"
    "v_and_b32 v237, s25, v158\n"
    "v_mfma_f32_16x16x32_bf16 v[28:31], a[24:27], a[172:175], v[28:31]\n"
    "v_lshlrev_b32 v238, 16, v159\n"
    "v_mfma_f32_16x16x32_bf16 v[28:31], a[28:31], a[188:191], v[28:31]\n"
    "v_and_b32 v239, s25, v159\n"
    "v_mfma_f32_16x16x32_bf16 v[44:47], a[32:35], a[140:143], v[44:47]\n"
    "ds_read_b128 a[208:211], v192 offset:4096\n"
    "ds_read_b128 a[212:215], v192 offset:5120\n"
    "v_lshlrev_b32 v232, 16, v156\n"
    "v_and_b32 v233, s25, v156\n"
    "v_lshlrev_b32 v234, 16, v157\n"
    "v_and_b32 v235, s25, v157\n"
    "v_pk_mul_f32 v[224:225], v[224:225], v[12:13]\n"
    "v_pk_mul_f32 v[226:227], v[226:227], v[14:15]\n"
    "v_pk_mul_f32 v[228:229], v[228:229], v[28:29]\n"
    "v_pk_mul_f32 v[230:231], v[230:231], v[30:31]\n"
    "v_mfma_f32_16x16x32_bf16 v[44:47], a[36:39], a[156:159], v[44:47]\n"
    "s_mov_b32 s23, 24576\n"
    "v_cvt_pk_bf16_f32 v224, v224, v225\n"
    "v_cvt_pk_bf16_f32 v225, v226, v227\n"
    "v_mfma_f32_16x16x32_bf16 v[44:47], a[48:51], a[172:175], v[44:47]\n"
    "buffer_store_dwordx4 a[204:207], %11, %3, s23 offen sc1\n"
    "v_mfma_f32_16x16x32_bf16 v[44:47], a[52:55], a[188:191], v[44:47]\n"
    "v_cvt_pk_bf16_f32 v226, v228, v229\n"
    "v_cvt_pk_bf16_f32 v227, v230, v231\n"
    "v_mfma_f32_16x16x32_bf16 v[60:63], a[40:43], a[140:143], v[60:63]\n"
    "ds_write_b128 v240, v[224:227] offset:6144\n"
    "v_mfma_f32_16x16x32_bf16 v[60:63], a[44:47], a[156:159], v[60:63]\n"
    "v_lshlrev_b32 v228, 16, v162\n"
    "v_mfma_f32_16x16x32_bf16 v[60:63], a[56:59], a[172:175], v[60:63]\n"
    "v_and_b32 v229, s25, v162\n"
    "v_lshlrev_b32 v230, 16, v163\n"
    "v_mfma_f32_16x16x32_bf16 v[60:63], a[60:63], a[188:191], v[60:63]\n"
    "v_and_b32 v231, s25, v163\n"
    "s_waitcnt vmcnt(4)\n"
    "v_mfma_f32_16x16x32_bf16 v[64:67], a[64:67], a[128:131], v[64:67]\n"
    "v_lshlrev_b32 v224, 16, v160\n"
    "v_and_b32 v225, s25, v160\n"
    "v_lshlrev_b32 v226, 16, v161\n"
    "v_and_b32 v227, s25, v161\n"
    "v_pk_mul_f32 v[232:233], v[232:233], v[44:45]\n"
    "v_pk_mul_f32 v[234:235], v[234:235], v[46:47]\n"
    "v_pk_mul_f32 v[236:237], v[236:237], v[60:61]\n"
    "v_pk_mul_f32 v[238:239], v[238:239], v[62:63]\n"
    "v_mfma_f32_16x16x32_bf16 v[64:67], a[68:71], a[144:147], v[64:67]\n"
    "s_mov_b32 s22, 32768\n"
    "v_cvt_pk_bf16_f32 v232, v232, v233\n"
    "v_cvt_pk_bf16_f32 v233, v234, v235\n"
    "v_mfma_f32_16x16x32_bf16 v[64:67], a[80:83], a[160:163], v[64:67]\n"
    "s_waitcnt lgkmcnt(1)\n"
    "buffer_store_dwordx4 a[208:211], %11, %3, s22 offen sc1\n"
    "v_mfma_f32_16x16x32_bf16 v[64:67], a[84:87], a[176:179], v[64:67]\n"
    "v_cvt_pk_bf16_f32 v234, v236, v237\n"
    "v_cvt_pk_bf16_f32 v235, v238, v239\n"
    "v_mfma_f32_16x16x32_bf16 v[80:83], a[72:75], a[128:131], v[80:83]\n"
    "ds_write_b128 v241, v[232:235] offset:6144\n"
    "v_mfma_f32_16x16x32_bf16 v[80:83], a[76:79], a[144:147], v[80:83]\n"
    "v_lshlrev_b32 v236, 16, v178\n"
    "v_mfma_f32_16x16x32_bf16 v[80:83], a[88:91], a[160:163], v[80:83]\n"
    "v_and_b32 v237, s25, v178\n"
    "v_lshlrev_b32 v238, 16, v179\n"
    "v_mfma_f32_16x16x32_bf16 v[80:83], a[92:95], a[176:179], v[80:83]\n"
    "v_and_b32 v239, s25, v179\n"
    "v_mfma_f32_16x16x32_bf16 v[96:99], a[96:99], a[128:131], v[96:99]\n"
    "ds_read_b128 a[216:219], v192 offset:6144\n"
    "ds_read_b128 a[220:223], v192 offset:7168\n"
    "v_lshlrev_b32 v232, 16, v176\n"
    "v_and_b32 v233, s25, v176\n"
    "v_lshlrev_b32 v234, 16, v177\n"
    "v_and_b32 v235, s25, v177\n"
    "v_pk_mul_f32 v[224:225], v[224:225], v[64:65]\n"
    "v_pk_mul_f32 v[226:227], v[226:227], v[66:67]\n"
    "v_pk_mul_f32 v[228:229], v[228:229], v[80:81]\n"
    "v_pk_mul_f32 v[230:231], v[230:231], v[82:83]\n"
    "v_mfma_f32_16x16x32_bf16 v[96:99], a[100:103], a[144:147], v[96:99]\n"
    "s_mov_b32 s23, 40960\n"
    "v_add_u32 v240, %4, %8\n"
    "v_cvt_pk_bf16_f32 v224, v224, v225\n"
    "v_cvt_pk_bf16_f32 v225, v226, v227\n"
    "v_mfma_f32_16x16x32_bf16 v[96:99], a[112:115], a[160:163], v[96:99]\n"
    "buffer_store_dwordx4 a[212:215], %11, %3, s23 offen sc1\n"
    "v_mfma_f32_16x16x32_bf16 v[96:99], a[116:119], a[176:179], v[96:99]\n"
    "v_cvt_pk_bf16_f32 v226, v228, v229\n"
    "v_cvt_pk_bf16_f32 v227, v230, v231\n"
    "v_mfma_f32_16x16x32_bf16 v[112:115], a[104:107], a[128:131], v[112:115]\n"
    "ds_write_b128 v240, v[224:227] offset:8192\n"
    "v_mfma_f32_16x16x32_bf16 v[112:115], a[108:111], a[144:147], v[112:115]\n"
    "v_lshlrev_b32 v228, 16, v166\n"
    "v_mfma_f32_16x16x32_bf16 v[112:115], a[120:123], a[160:163], v[112:115]\n"
    "v_and_b32 v229, s25, v166\n"
    "v_lshlrev_b32 v230, 16, v167\n"
    "v_mfma_f32_16x16x32_bf16 v[112:115], a[124:127], a[176:179], v[112:115]\n"
    "v_and_b32 v231, s25, v167\n"
    "v_mfma_f32_16x16x32_bf16 v[68:71], a[64:67], a[132:135], v[68:71]\n"
    "v_lshlrev_b32 v224, 16, v164\n"
    "v_and_b32 v225, s25, v164\n"
    "v_lshlrev_b32 v226, 16, v165\n"
    "v_and_b32 v227, s25, v165\n"
    "v_pk_mul_f32 v[232:233], v[232:233], v[96:97]\n"
    "v_pk_mul_f32 v[234:235], v[234:235], v[98:99]\n"
    "v_pk_mul_f32 v[236:237], v[236:237], v[112:113]\n"
    "v_pk_mul_f32 v[238:239], v[238:239], v[114:115]\n"
    "v_mfma_f32_16x16x32_bf16 v[68:71], a[68:71], a[148:151], v[68:71]\n"
    "s_mov_b32 s22, 49152\n"
    "v_add_u32 v241, %4, %9\n"
    "v_cvt_pk_bf16_f32 v232, v232, v233\n"
    "v_cvt_pk_bf16_f32 v233, v234, v235\n"
    "v_mfma_f32_16x16x32_bf16 v[68:71], a[80:83], a[164:167], v[68:71]\n"
    "s_waitcnt lgkmcnt(1)\n"
    "buffer_store_dwordx4 a[216:219], %11, %3, s22 offen sc1\n"
    "v_mfma_f32_16x16x32_bf16 v[68:71], a[84:87], a[180:183], v[68:71]\n"
    "v_cvt_pk_bf16_f32 v234, v236, v237\n"
    "v_cvt_pk_bf16_f32 v235, v238, v239\n"
    "v_mfma_f32_16x16x32_bf16 v[84:87], a[72:75], a[132:135], v[84:87]\n"
    "ds_write_b128 v241, v[232:235] offset:8192\n"
    "v_mfma_f32_16x16x32_bf16 v[84:87], a[76:79], a[148:151], v[84:87]\n"
    "v_lshlrev_b32 v236, 16, v182\n"
    "v_mfma_f32_16x16x32_bf16 v[84:87], a[88:91], a[164:167], v[84:87]\n"
    "v_and_b32 v237, s25, v182\n"
    "v_lshlrev_b32 v238, 16, v183\n"
    "v_mfma_f32_16x16x32_bf16 v[84:87], a[92:95], a[180:183], v[84:87]\n"
    "v_and_b32 v239, s25, v183\n"
    "v_mfma_f32_16x16x32_bf16 v[100:103], a[96:99], a[132:135], v[100:103]\n"
    "ds_read_b128 a[224:227], v192 offset:8192\n"
    "ds_read_b128 a[228:231], v192 offset:9216\n"
    "v_lshlrev_b32 v232, 16, v180\n"
    "v_and_b32 v233, s25, v180\n"
    "v_lshlrev_b32 v234, 16, v181\n"
    "v_and_b32 v235, s25, v181\n"
    "v_pk_mul_f32 v[224:225], v[224:225], v[68:69]\n"
    "v_pk_mul_f32 v[226:227], v[226:227], v[70:71]\n"
    "v_pk_mul_f32 v[228:229], v[228:229], v[84:85]\n"
    "v_pk_mul_f32 v[230:231], v[230:231], v[86:87]\n"
    "v_mfma_f32_16x16x32_bf16 v[100:103], a[100:103], a[148:151], v[100:103]\n"
    "s_mov_b32 s23, 57344\n"
    "v_cvt_pk_bf16_f32 v224, v224, v225\n"
    "v_cvt_pk_bf16_f32 v225, v226, v227\n"
    "v_mfma_f32_16x16x32_bf16 v[100:103], a[112:115], a[164:167], v[100:103]\n"
    "buffer_store_dwordx4 a[220:223], %11, %3, s23 offen sc1\n"
    "v_mfma_f32_16x16x32_bf16 v[100:103], a[116:119], a[180:183], v[100:103]\n"
    "v_cvt_pk_bf16_f32 v226, v228, v229\n"
    "v_cvt_pk_bf16_f32 v227, v230, v231\n"
    "v_mfma_f32_16x16x32_bf16 v[116:119], a[104:107], a[132:135], v[116:119]\n"
    "ds_write_b128 v240, v[224:227] offset:10240\n"
    "v_mfma_f32_16x16x32_bf16 v[116:119], a[108:111], a[148:151], v[116:119]\n"
    "v_lshlrev_b32 v228, 16, v170\n"
    "v_mfma_f32_16x16x32_bf16 v[116:119], a[120:123], a[164:167], v[116:119]\n"
    "v_and_b32 v229, s25, v170\n"
    "v_lshlrev_b32 v230, 16, v171\n"
    "v_mfma_f32_16x16x32_bf16 v[116:119], a[124:127], a[180:183], v[116:119]\n"
    "v_and_b32 v231, s25, v171\n"
    "v_mfma_f32_16x16x32_bf16 v[72:75], a[64:67], a[136:139], v[72:75]\n"
    "v_lshlrev_b32 v224, 16, v168\n"
    "v_and_b32 v225, s25, v168\n"
    "v_lshlrev_b32 v226, 16, v169\n"
    "v_and_b32 v227, s25, v169\n"
    "v_pk_mul_f32 v[232:233], v[232:233], v[100:101]\n"
    "v_pk_mul_f32 v[234:235], v[234:235], v[102:103]\n"
    "v_pk_mul_f32 v[236:237], v[236:237], v[116:117]\n"
    "v_pk_mul_f32 v[238:239], v[238:239], v[118:119]\n"
    "v_mfma_f32_16x16x32_bf16 v[72:75], a[68:71], a[152:155], v[72:75]\n"
    "s_mov_b32 s22, 128\n"
    "v_cvt_pk_bf16_f32 v232, v232, v233\n"
    "v_cvt_pk_bf16_f32 v233, v234, v235\n"
    "v_mfma_f32_16x16x32_bf16 v[72:75], a[80:83], a[168:171], v[72:75]\n"
    "s_waitcnt lgkmcnt(1)\n"
    "buffer_store_dwordx4 a[224:227], %11, %3, s22 offen sc1\n"
    "v_mfma_f32_16x16x32_bf16 v[72:75], a[84:87], a[184:187], v[72:75]\n"
    "v_cvt_pk_bf16_f32 v234, v236, v237\n"
    "v_cvt_pk_bf16_f32 v235, v238, v239\n"
    "v_mfma_f32_16x16x32_bf16 v[88:91], a[72:75], a[136:139], v[88:91]\n"
    "ds_write_b128 v241, v[232:235] offset:10240\n"
    "v_mfma_f32_16x16x32_bf16 v[88:91], a[76:79], a[152:155], v[88:91]\n"
    "v_lshlrev_b32 v236, 16, v186\n"
    "v_mfma_f32_16x16x32_bf16 v[88:91], a[88:91], a[168:171], v[88:91]\n"
    "v_and_b32 v237, s25, v186\n"
    "v_lshlrev_b32 v238, 16, v187\n"
    "v_mfma_f32_16x16x32_bf16 v[88:91], a[92:95], a[184:187], v[88:91]\n"
    "v_and_b32 v239, s25, v187\n"
    "v_mfma_f32_16x16x32_bf16 v[104:107], a[96:99], a[136:139], v[104:107]\n"
    "ds_read_b128 a[232:235], v192 offset:10240\n"
    "ds_read_b128 a[236:239], v192 offset:11264\n"
    "v_lshlrev_b32 v232, 16, v184\n"
    "v_and_b32 v233, s25, v184\n"
    "v_lshlrev_b32 v234, 16, v185\n"
    "v_and_b32 v235, s25, v185\n"
    "v_pk_mul_f32 v[224:225], v[224:225], v[72:73]\n"
    "v_pk_mul_f32 v[226:227], v[226:227], v[74:75]\n"
    "v_pk_mul_f32 v[228:229], v[228:229], v[88:89]\n"
    "v_pk_mul_f32 v[230:231], v[230:231], v[90:91]\n"
    "v_mfma_f32_16x16x32_bf16 v[104:107], a[100:103], a[152:155], v[104:107]\n"
    "s_mov_b32 s23, 8320\n"
    "v_cvt_pk_bf16_f32 v224, v224, v225\n"
    "v_cvt_pk_bf16_f32 v225, v226, v227\n"
    "v_mfma_f32_16x16x32_bf16 v[104:107], a[112:115], a[168:171], v[104:107]\n"
    "buffer_store_dwordx4 a[228:231], %11, %3, s23 offen sc1\n"
    "v_mfma_f32_16x16x32_bf16 v[104:107], a[116:119], a[184:187], v[104:107]\n"
    "v_cvt_pk_bf16_f32 v226, v228, v229\n"
    "v_cvt_pk_bf16_f32 v227, v230, v231\n"
    "v_mfma_f32_16x16x32_bf16 v[120:123], a[104:107], a[136:139], v[120:123]\n"
    "ds_write_b128 v240, v[224:227] offset:12288\n"
    "v_mfma_f32_16x16x32_bf16 v[120:123], a[108:111], a[152:155], v[120:123]\n"
    "v_lshlrev_b32 v228, 16, v174\n"
    "v_mfma_f32_16x16x32_bf16 v[120:123], a[120:123], a[168:171], v[120:123]\n"
    "v_and_b32 v229, s25, v174\n"
    "v_lshlrev_b32 v230, 16, v175\n"
    "v_mfma_f32_16x16x32_bf16 v[120:123], a[124:127], a[184:187], v[120:123]\n"
    "v_and_b32 v231, s25, v175\n"
    "v_mfma_f32_16x16x32_bf16 v[76:79], a[64:67], a[140:143], v[76:79]\n"
    "v_lshlrev_b32 v224, 16, v172\n"
    "v_and_b32 v225, s25, v172\n"
    "v_lshlrev_b32 v226, 16, v173\n"
    "v_and_b32 v227, s25, v173\n"
    "v_pk_mul_f32 v[232:233], v[232:233], v[104:105]\n"
    "v_pk_mul_f32 v[234:235], v[234:235], v[106:107]\n"
    "v_pk_mul_f32 v[236:237], v[236:237], v[120:121]\n"
    "v_pk_mul_f32 v[238:239], v[238:239], v[122:123]\n"
    "v_mfma_f32_16x16x32_bf16 v[76:79], a[68:71], a[156:159], v[76:79]\n"
    "s_mov_b32 s22, 16512\n"
    "v_cvt_pk_bf16_f32 v232, v232, v233\n"
    "v_cvt_pk_bf16_f32 v233, v234, v235\n"
    "v_mfma_f32_16x16x32_bf16 v[76:79], a[80:83], a[172:175], v[76:79]\n"
    "s_waitcnt lgkmcnt(1)\n"
    "buffer_store_dwordx4 a[232:235], %11, %3, s22 offen sc1\n"
    "v_mfma_f32_16x16x32_bf16 v[76:79], a[84:87], a[188:191], v[76:79]\n"
    "v_cvt_pk_bf16_f32 v234, v236, v237\n"
    "v_cvt_pk_bf16_f32 v235, v238, v239\n"
    "v_mfma_f32_16x16x32_bf16 v[92:95], a[72:75], a[140:143], v[92:95]\n"
    "ds_write_b128 v241, v[232:235] offset:12288\n"
    "v_mfma_f32_16x16x32_bf16 v[92:95], a[76:79], a[156:159], v[92:95]\n"
    "v_lshlrev_b32 v236, 16, v190\n"
    "v_mfma_f32_16x16x32_bf16 v[92:95], a[88:91], a[172:175], v[92:95]\n"
    "v_and_b32 v237, s25, v190\n"
    "v_lshlrev_b32 v238, 16, v191\n"
    "v_mfma_f32_16x16x32_bf16 v[92:95], a[92:95], a[188:191], v[92:95]\n"
    "v_and_b32 v239, s25, v191\n"
    "v_mfma_f32_16x16x32_bf16 v[108:111], a[96:99], a[140:143], v[108:111]\n"
    "ds_read_b128 a[192:195], v192 offset:12288\n"
    "ds_read_b128 a[196:199], v192 offset:13312\n"
    "v_lshlrev_b32 v232, 16, v188\n"
    "v_and_b32 v233, s25, v188\n"
    "v_lshlrev_b32 v234, 16, v189\n"
    "v_and_b32 v235, s25, v189\n"
    "v_pk_mul_f32 v[224:225], v[224:225], v[76:77]\n"
    "v_pk_mul_f32 v[226:227], v[226:227], v[78:79]\n"
    "v_pk_mul_f32 v[228:229], v[228:229], v[92:93]\n"
    "v_pk_mul_f32 v[230:231], v[230:231], v[94:95]\n"
    "v_mfma_f32_16x16x32_bf16 v[108:111], a[100:103], a[156:159], v[108:111]\n"
    "s_mov_b32 s23, 24704\n"
    "v_cvt_pk_bf16_f32 v224, v224, v225\n"
    "v_cvt_pk_bf16_f32 v225, v226, v227\n"
    "v_mfma_f32_16x16x32_bf16 v[108:111], a[112:115], a[172:175], v[108:111]\n"
    "buffer_store_dwordx4 a[236:239], %11, %3, s23 offen sc1\n"
    "v_mfma_f32_16x16x32_bf16 v[108:111], a[116:119], a[188:191], v[108:111]\n"
    "v_cvt_pk_bf16_f32 v226, v228, v229\n"
    "v_cvt_pk_bf16_f32 v227, v230, v231\n"
    "v_mfma_f32_16x16x32_bf16 v[124:127], a[104:107], a[140:143], v[124:127]\n"
    "ds_write_b128 v240, v[224:227] offset:14336\n"
    "v_mfma_f32_16x16x32_bf16 v[124:127], a[108:111], a[156:159], v[124:127]\n"
    "v_mfma_f32_16x16x32_bf16 v[124:127], a[120:123], a[172:175], v[124:127]\n"
    "s_waitcnt lgkmcnt(1)\n"
    "v_mfma_f32_16x16x32_bf16 v[124:127], a[124:127], a[188:191], v[124:127]\n"
    "s_mov_b32 s22, 32896\n"
    "buffer_store_dwordx4 a[192:195], %11, %3, s22 offen sc1\n"
    "v_pk_mul_f32 v[232:233], v[232:233], v[108:109]\n"
    "v_pk_mul_f32 v[234:235], v[234:235], v[110:111]\n"
    "s_nop 3\n"
    "v_pk_mul_f32 v[236:237], v[236:237], v[124:125]\n"
    "v_pk_mul_f32 v[238:239], v[238:239], v[126:127]\n"
    "s_mov_b32 s23, 41088\n"
    "buffer_store_dwordx4 a[196:199], %11, %3, s23 offen sc1\n"
    "v_cvt_pk_bf16_f32 v232, v232, v233\n"
    "v_cvt_pk_bf16_f32 v233, v234, v235\n"
    "v_cvt_pk_bf16_f32 v234, v236, v237\n"
    "v_cvt_pk_bf16_f32 v235, v238, v239\n"
    "ds_write_b128 v241, v[232:235] offset:14336\n"
    "ds_read_b128 a[200:203], v192 offset:14336\n"
    "ds_read_b128 a[204:207], v192 offset:15360\n"
    "s_mov_b32 s22, 49280\n"
    "s_waitcnt lgkmcnt(1)\n"
    "buffer_store_dwordx4 a[200:203], %11, %3, s22 offen sc1\n"
    "s_mov_b32 s23, 57472\n"
    "s_waitcnt lgkmcnt(0)\n"
    "buffer_store_dwordx4 a[204:207], %11, %3, s23 offen sc1\n"
    ".Ldcn_end%=:\n"
    "s_nop 0\n"
    : "+v"(boff), "+v"(fwo)
    : "s"(sW), "s"(sO), "s"(w * 16384), "s"(w * 4096), "s"(pfV), "s"(lbase + SCR + w * 256),
      "v"(xo0), "v"(xo1), "v"(hoff), "v"(voX),
      "s"(sX), "s"(lbase + XIMG + w * 16384),
      "s"(sS), "s"(sC), "s"(sE), "s"(sF), "s"((w + 1) * W_LAYER + srcoff), "s"((w + 1) * 262144 + blk * 1024 - w * 65536), "s"(4096 + w * 512 - (hc >> 1)),
      "s"(sV0), "s"(sU), "s"(ring),
      "v"(voV), "v"(voU), "v"(aV0), "v"(aV1), "v"(aU0), "v"(aU1)
    : "v0", "v1", "v2", "v3", "v4", "v5", "v6", "v7", "v8", "v9", "v10", "v11", "v12", "v13", "v14", "v15", "v16", "v17", "v18", "v19", "v20", "v21", "v22", "v23", "v24", "v25", "v26", "v27", "v28", "v29", "v30", "v31", "v32", "v33", "v34", "v35", "v36", "v37", "v38", "v39", "v40", "v41", "v42", "v43", "v44", "v45", "v46", "v47", "v48", "v49", "v50", "v51", "v52", "v53", "v54", "v55", "v56", "v57", "v58", "v59", "v60", "v61", "v62", "v63", "v64", "v65", "v66", "v67", "v68", "v69", "v70", "v71", "v72", "v73", "v74", "v75", "v76", "v77", "v78", "v79", "v80", "v81", "v82", "v83", "v84", "v85", "v86", "v87", "v88", "v89", "v90", "v91", "v92", "v93", "v94", "v95", "v96", "v97", "v98", "v99", "v100", "v101", "v102", "v103", "v104", "v105", "v106", "v107", "v108", "v109", "v110", "v111", "v112", "v113", "v114", "v115", "v116", "v117", "v118", "v119", "v120", "v121", "v122", "v123", "v124", "v125", "v126", "v127", "v128", "v129", "v130", "v131", "v132", "v133", "v134", "v135", "v136", "v137", "v138", "v139", "v140", "v141", "v142", "v143", "v144", "v145", "v146", "v147", "v148", "v149", "v150", "v151", "v152", "v153", "v154", "v155", "v156", "v157", "v158", "v159", "v160", "v161", "v162", "v163", "v164", "v165", "v166", "v167", "v168", "v169", "v170", "v171", "v172", "v173", "v174", "v175", "v176", "v177", "v178", "v179", "v180", "v181", "v182", "v183", "v184", "v185", "v186", "v187", "v188", "v189", "v190", "v191", "v192", "v193", "v194", "v195", "v196", "v197", "v198", "v199", "v200", "v201", "v202", "v203", "v204", "v205", "v206", "v207", "v208", "v209", "v210", "v211", "v212", "v213", "v214", "v215", "v216", "v217", "v218", "v219", "v220", "v221", "v222", "v223", "v224", "v225", "v226", "v227", "v228", "v229", "v230", "v231", "v232", "v233", "v234", "v235", "v236", "v237", "v238", "v239", "v240", "v241", "v242", "a0", "a1", "a2", "a3", "a4", "a5", "a6", "a7", "a8", "a9", "a10", "a11", "a12", "a13", "a14", "a15", "a16", "a17", "a18", "a19", "a20", "a21", "a22", "a23", "a24", "a25", "a26", "a27", "a28", "a29", "a30", "a31", "a32", "a33", "a34", "a35", "a36", "a37", "a38", "a39", "a40", "a41", "a42", "a43", "a44", "a45", "a46", "a47", "a48", "a49", "a50", "a51", "a52", "a53", "a54", "a55", "a56", "a57", "a58", "a59", "a60", "a61", "a62", "a63", "a64", "a65", "a66", "a67", "a68", "a69", "a70", "a71", "a72", "a73", "a74", "a75", "a76", "a77", "a78", "a79", "a80", "a81", "a82", "a83", "a84", "a85", "a86", "a87", "a88", "a89", "a90", "a91", "a92", "a93", "a94", "a95", "a96", "a97", "a98", "a99", "a100", "a101", "a102", "a103", "a104", "a105", "a106", "a107", "a108", "a109", "a110", "a111", "a112", "a113", "a114", "a115", "a116", "a117", "a118", "a119", "a120", "a121", "a122", "a123", "a124", "a125", "a126", "a127", "a128", "a129", "a130", "a131", "a132", "a133", "a134", "a135", "a136", "a137", "a138", "a139", "a140", "a141", "a142", "a143", "a144", "a145", "a146", "a147", "a148", "a149", "a150", "a151", "a152", "a153", "a154", "a155", "a156", "a157", "a158", "a159", "a160", "a161", "a162", "a163", "a164", "a165", "a166", "a167", "a168", "a169", "a170", "a171", "a172", "a173", "a174", "a175", "a176", "a177", "a178", "a179", "a180", "a181", "a182", "a183", "a184", "a185", "a186", "a187", "a188", "a189", "a190", "a191", "a192", "a193", "a194", "a195", "a196", "a197", "a198", "a199", "a200", "a201", "a202", "a203", "a204", "a205", "a206", "a207", "a208", "a209", "a210", "a211", "a212", "a213", "a214", "a215", "a216", "a217", "a218", "a219", "a220", "a221", "a222", "a223", "a224", "a225", "a226", "a227", "a228", "a229", "a230", "a231", "a232", "a233", "a234", "a235", "a236", "a237", "a238", "a239", "a240", "a241", "a242", "a243", "a244", "a245", "a246", "a247", "a248", "a249", "a250", "a251", "a252", "a253", "s20", "s21", "s22", "s23", "s24", "s25", "s26", "s27", "s28", "s29", "vcc", "scc", "memory");

}

}

extern "C" void kernel_launch(void* const* d_in, const int* in_sizes, int n_in, void* d_out, int out_size, void* d_ws,
                              size_t ws_size, hipStream_t stream) {
  const unsigned char* x0 = (const unsigned char*)d_in[0];
  const unsigned char* U = (const unsigned char*)d_in[1];
  const unsigned char* V = (const unsigned char*)d_in[2];
  const unsigned char* bs = (const unsigned char*)d_in[3];
  const int rows = in_sizes[0] / kD;
  (void)hipFuncSetAttribute((const void*)dcn_kernel, hipFuncAttributeMaxDynamicSharedMemorySize, LDS_BYTES);
  dcn_kernel<<<rows / kM, 256, LDS_BYTES, stream>>>(x0, U, V, (unsigned char*)d_ws, bs, (unsigned char*)d_out);
}
```
